```python
import torch

def _softmax(x, axis=-1):
    return torch.softmax(x, dim=axis)
def _gelu(x):
    return torch.nn.functional.gelu(x, approximate="tanh")
def _silu(x):
    return torch.nn.functional.silu(x)
def _sigmoid(x):
    return torch.sigmoid(x)
def _layernorm(x, axis=-1, eps=1e-5):
    m = x.mean(dim=axis, keepdim=True)
    v = x.var(dim=axis, keepdim=True, unbiased=False)
    return (x - m) / torch.sqrt(v + eps)
def _scatter_add(tgt, idx, src, axis=0):
    return tgt.clone().index_add_(axis, idx, src)
def _segment_sum(src, seg, n):
    out = torch.zeros((n,) + src.shape[1:], dtype=src.dtype)
    return out.index_add_(0, seg, src)
def _pad(x, pw):
    flat = tuple(v for pair in reversed(pw) for v in pair)
    return torch.nn.functional.pad(x, flat)

kernel_name = 'sequential_preLN__mha_block_cached__geglu_ffn__decode'

def setup_inputs():

    _w = (lambda a: (lambda t: t.to(torch.bfloat16) if t.is_floating_point() else t)(torch.as_tensor(a)))
    def _A_setup():
        B, Sq, D = 8, 1, 640
        H, Sk, Dh = 32, 4096, 128
        return {
            "x": _w((lambda *s: torch.randn(*s, dtype=torch.bfloat16) * 0.05)(B, Sq, D)),
            "k_cache": _w((lambda *s: torch.randn(*s, dtype=torch.bfloat16) * 3.2)(B, H, Sk, Dh)),
            "v_cache": _w((lambda *s: torch.randn(*s, dtype=torch.bfloat16) * 0.4)(B, H, Sk, Dh)),
            "wq": _w((lambda *s: torch.randn(*s, dtype=torch.bfloat16) * 3.2)(D, H * Dh) * (D ** -0.5)),
            "wo": _w((lambda *s: torch.randn(*s, dtype=torch.bfloat16) * 0.4)(H * Dh, D) * ((H * Dh) ** -0.5)),
        }
    _A_in = _A_setup()

    def _B_setup():
        B, S, D, F = 8, 1, 640, 1728
        return {
            "x": _w((lambda *s: torch.randn(*s, dtype=torch.bfloat16) * 0.05)(B, S, D)),
            "w_gate": _w((lambda *s: torch.randn(*s, dtype=torch.bfloat16) * 0.05)(D, F)),
            "w_up": _w((lambda *s: torch.randn(*s, dtype=torch.bfloat16) * 0.05)(D, F)),
            "w_down": _w((lambda *s: torch.randn(*s, dtype=torch.bfloat16) * 0.05)(F, D)),
        }
    _B_in = _B_setup()

    return {
        "A_x": _A_in["x"],
        "A_k_cache": _A_in["k_cache"],
        "A_v_cache": _A_in["v_cache"],
        "A_wq": _A_in["wq"],
        "A_wo": _A_in["wo"],
        "B_x": _B_in["x"],
        "B_w_gate": _B_in["w_gate"],
        "B_w_up": _B_in["w_up"],
        "B_w_down": _B_in["w_down"],
    }

def reference(A_x, A_k_cache, A_v_cache, A_wq, A_wo, B_x, B_w_gate, B_w_up, B_w_down):
    def _A_ref(x, k_cache, v_cache, wq, wo):
        B, Sq, D = x.shape
        H, Dh = 32, 128
        q = (lambda x, *d: x.permute(*d))((x @ wq).reshape(B, Sq, H, Dh), 0, 2, 1, 3)
        scores = torch.einsum("bhqd,bhkd->bhqk", q, k_cache) * (Dh ** -0.5)
        p = _softmax(scores, axis=-1)
        o = torch.einsum("bhqk,bhkd->bhqd", p, v_cache)
        return (lambda x, *d: x.permute(*d))(o, 0, 2, 1, 3).reshape(B, Sq, H * Dh) @ wo

    def _B_ref(x, w_gate, w_up, w_down):
        return (_gelu(x @ w_gate) * (x @ w_up)) @ w_down

    x = A_x
    h = x + _A_ref(_layernorm(x, axis=-1), A_k_cache, A_v_cache, A_wq, A_wo)
    h2 = h + _B_ref(_layernorm(h, axis=-1), B_w_gate, B_w_up, B_w_down)
    return h2
```

```cpp
#include <hip/hip_runtime.h>
#include <hip/hip_bf16.h>
#include <stdint.h>

#define MIMIC 1

namespace {
constexpr int B = 8, D = 640, H = 32, SK = 4096, DH = 128, F = 1728;
typedef unsigned u32x4 __attribute__((ext_vector_type(4)));
typedef float f32x4 __attribute__((ext_vector_type(4)));
typedef __bf16 bf16x2 __attribute__((ext_vector_type(2)));

__device__ __forceinline__ float bf_lo(unsigned u) { return __builtin_bit_cast(float, u << 16); }
__device__ __forceinline__ float bf_hi(unsigned u) { return __builtin_bit_cast(float, u & 0xffff0000u); }
__device__ __forceinline__ float bf2f(unsigned short u) { return __builtin_bit_cast(float, (unsigned)u << 16); }
__device__ __forceinline__ unsigned short f2bf(float f) {
  __bf16 b = (__bf16)f;
  return __builtin_bit_cast(unsigned short, b);
}
__device__ __forceinline__ float rbf(float f) {
  return bf2f(f2bf(f));
}
__device__ __forceinline__ float dot2(unsigned k, unsigned q, float acc) {
  return __builtin_amdgcn_fdot2_f32_bf16(__builtin_bit_cast(bf16x2, k), __builtin_bit_cast(bf16x2, q), acc, false);
}
template <int CTRL> __device__ __forceinline__ float dpp(float x) {
  return __builtin_bit_cast(float, __builtin_amdgcn_mov_dpp(__builtin_bit_cast(int, x), CTRL, 0xf, 0xf, true));
}
constexpr int XOR1 = 0xB1, XOR2 = 0x4E, XOR7 = 0x141, XOR8 = 0x128;
__device__ __forceinline__ float row16_sum(float x) {
  x += dpp<XOR1>(x); x += dpp<XOR2>(x); x += dpp<XOR7>(x); x += dpp<XOR8>(x); return x;
}
__device__ __forceinline__ float wave_sum(float x) {
  x = row16_sum(x);
  x += __shfl_xor(x, 16); x += __shfl_xor(x, 32);
  return x;
}

constexpr int NW = 8, NT = NW * 64;
constexpr int U = 4;
constexpr int TOK_PER_TILE = 4 * U;
constexpr int NTILE = SK / (TOK_PER_TILE * NW);

__global__ void __launch_bounds__(NT, 2) k_attn(const unsigned short* __restrict__ x, const unsigned short* __restrict__ kc,
                                                const unsigned short* __restrict__ vc, const unsigned short* __restrict__ wq,
                                                const unsigned short* __restrict__ wo, float* __restrict__ part) {
  __shared__ __attribute__((aligned(16))) float lds[8192];
  const int bid = blockIdx.x, xcd = bid & 7, j = bid >> 3;
  const int h = xcd * 4 + (j & 3), b = j >> 2;
  const int tid = threadIdx.x, lane = tid & 63, w = __builtin_amdgcn_readfirstlane(tid >> 6), r = lane >> 4, c = lane & 15;

  const u32x4* kp = (const u32x4*)(kc + (size_t)(b * H + h) * SK * DH) + (size_t)w * (U * 64) + lane;
  const u32x4* vp = (const u32x4*)(vc + (size_t)(b * H + h) * SK * DH) + (size_t)w * (U * 64) + lane;

  float* xn = lds;
  float* red = lds + 1024;
  {
    float xv[10];
    float s = 0.f;
#pragma unroll
    for (int i = 0; i < 10; ++i) { xv[i] = bf2f(x[b * D + lane + 64 * i]); s += xv[i]; }
    s = wave_sum(s);
    const float mean = s * (1.f / D);
    float v = 0.f;
#pragma unroll
    for (int i = 0; i < 10; ++i) { const float d = xv[i] - mean; v += d * d; }
    v = wave_sum(v) * (1.f / D);
    const float mb = rbf(mean), vb = rbf(v);
    const float den = rbf(sqrtf(rbf(vb + 1e-5f)));
    if (w == 0) {
#pragma unroll
      for (int i = 0; i < 10; ++i) xn[lane + 64 * i] = rbf(rbf(xv[i] - mb) / den);
    }
  }
  __syncthreads();
  unsigned qpk[4];
  {
    const int rg = tid >> 4;
    float a[8];
#pragma unroll
    for (int k = 0; k < 8; ++k) a[k] = 0.f;
#pragma unroll
    for (int i = 0; i < 20; ++i) {
      const int row = rg + 32 * i;
      const u32x4 wv = *(const u32x4*)(wq + (size_t)row * (H * DH) + h * DH + 8 * c);
      const float xs = xn[row];
#pragma unroll
      for (int k = 0; k < 4; ++k) { a[2 * k] = fmaf(xs, bf_lo(wv[k]), a[2 * k]); a[2 * k + 1] = fmaf(xs, bf_hi(wv[k]), a[2 * k + 1]); }
    }
#pragma unroll
    for (int k = 0; k < 8; ++k) { a[k] += __shfl_xor(a[k], 16); a[k] += __shfl_xor(a[k], 32); }
    if (r == 0) {
#pragma unroll
      for (int k = 0; k < 8; ++k) red[w * 128 + 8 * c + k] = a[k];
    }
    __syncthreads();
    float q[8];
#pragma unroll
    for (int k = 0; k < 8; ++k) {
      float s = 0.f;
#pragma unroll
      for (int ww = 0; ww < NW; ++ww) s += red[ww * 128 + 8 * c + k];
      q[k] = s;
    }
#pragma unroll
    for (int k = 0; k < 4; ++k) qpk[k] = (unsigned)f2bf(q[2 * k]) | ((unsigned)f2bf(q[2 * k + 1]) << 16);
  }

  constexpr float SCALE = 0.08838834764831845f, LOG2E = 1.4426950408889634f;
  float m = -INFINITY, l = 0.f, acc[8];
#pragma unroll
  for (int k = 0; k < 8; ++k) acc[k] = 0.f;
  u32x4 Kn[U], Vn[U];
#pragma unroll
  for (int u = 0; u < U; ++u) Kn[u] = __builtin_nontemporal_load(kp + u * 64);
#pragma unroll
  for (int u = 0; u < U; ++u) Vn[u] = __builtin_nontemporal_load(vp + u * 64);
  for (int t = 0; t < NTILE; ++t) {
    u32x4 Kc[U], Vc[U];
#pragma unroll
    for (int u = 0; u < U; ++u) { Kc[u] = Kn[u]; Vc[u] = Vn[u]; }
    if (t + 1 < NTILE) {
      const size_t off = (size_t)(t + 1) * (NW * U * 64);
#pragma unroll
      for (int u = 0; u < U; ++u) Kn[u] = __builtin_nontemporal_load(kp + off + u * 64);
#pragma unroll
      for (int u = 0; u < U; ++u) Vn[u] = __builtin_nontemporal_load(vp + off + u * 64);
    }
    float s[U];
#pragma unroll
    for (int u = 0; u < U; ++u) {
      float d = 0.f;
#pragma unroll
      for (int k = 0; k < 4; ++k) d = dot2(Kc[u][k], qpk[k], d);
      d = row16_sum(d);
      s[u] = rbf(rbf(d) * SCALE) * LOG2E;
    }
    float mn = m;
#pragma unroll
    for (int u = 0; u < U; ++u) mn = fmaxf(mn, s[u]);
    const float alpha = __builtin_amdgcn_exp2f(m - mn);
    m = mn;
    l *= alpha;
#pragma unroll
    for (int k = 0; k < 8; ++k) acc[k] *= alpha;
#pragma unroll
    for (int u = 0; u < U; ++u) {
      const float p = __builtin_amdgcn_exp2f(s[u] - mn);
      l += p;
#pragma unroll
      for (int k = 0; k < 4; ++k) { acc[2 * k] = fmaf(p, bf_lo(Vc[u][k]), acc[2 * k]); acc[2 * k + 1] = fmaf(p, bf_hi(Vc[u][k]), acc[2 * k + 1]); }
    }
  }
  __syncthreads();
  float* mm = lds;
  float* ll = lds + 64;
  float* aa = lds + 128;
  {
    const int g = w * 4 + r;
    if (c == 0) { mm[g] = m; ll[g] = l; }
    *(f32x4*)(aa + g * 128 + 8 * c) = f32x4{acc[0], acc[1], acc[2], acc[3]};
    *(f32x4*)(aa + g * 128 + 8 * c + 4) = f32x4{acc[4], acc[5], acc[6], acc[7]};
  }
  __syncthreads();
  float* ov = lds + 128 + 32 * 128;
  if (tid < 128) {
    float M = -INFINITY;
#pragma unroll
    for (int g = 0; g < 32; ++g) M = fmaxf(M, mm[g]);
    float L = 0.f, o = 0.f;
#pragma unroll
    for (int g = 0; g < 32; ++g) { const float wt = __builtin_amdgcn_exp2f(mm[g] - M); L = fmaf(wt, ll[g], L); o = fmaf(wt, aa[g * 128 + tid], o); }
    ov[tid] = rbf(o / L);
  }
  __syncthreads();
  if (tid < 320) {
    float p0 = 0.f, p1 = 0.f;
    const unsigned* wp = (const unsigned*)(wo + (size_t)h * DH * D) + tid;
#pragma unroll 16
    for (int k = 0; k < DH; ++k) { const unsigned wv = wp[k * 320]; const float o = ov[k]; p0 = fmaf(o, bf_lo(wv), p0); p1 = fmaf(o, bf_hi(wv), p1); }
    *(float2*)(part + (size_t)(b * H + h) * D + 2 * tid) = make_float2(p0, p1);
  }
}

__global__ void __launch_bounds__(512) k_gateup(const unsigned short* __restrict__ x, const float* __restrict__ part,
                                                const unsigned short* __restrict__ wg, const unsigned short* __restrict__ wu,
                                                float* __restrict__ hbuf, float* __restrict__ act) {
  __shared__ __attribute__((aligned(16))) float hn[B * D];
  __shared__ __attribute__((aligned(16))) float red[8][2][B][16];
  const int tid = threadIdx.x, lane = tid & 63, w = tid >> 6;
  {
    float hv[10]; float s = 0.f;
#pragma unroll
    for (int i = 0; i < 10; ++i) {
      const int col = lane + 64 * i;
      float a = 0.f;
#pragma unroll
      for (int hh = 0; hh < H; ++hh) a += part[(size_t)(w * H + hh) * D + col];
      hv[i] = rbf(bf2f(x[w * D + col]) + rbf(a));
      s += hv[i];
    }
    s = wave_sum(s);
    const float mean = s * (1.f / D);
    float v = 0.f;
#pragma unroll
    for (int i = 0; i < 10; ++i) { const float d = hv[i] - mean; v += d * d; }
    v = wave_sum(v) * (1.f / D);
    const float mb = rbf(mean), vb = rbf(v);
    const float den = rbf(sqrtf(rbf(vb + 1e-5f)));
#pragma unroll
    for (int i = 0; i < 10; ++i) {
      hn[w * D + lane + 64 * i] = rbf(rbf(hv[i] - mb) / den);
      if (blockIdx.x == 0) hbuf[w * D + lane + 64 * i] = hv[i];
    }
  }
  __syncthreads();
  const int cp = tid & 7, which = (tid >> 3) & 1, rg = tid >> 4;
  const unsigned short* wm = which ? wu : wg;
  float a0[B], a1[B];
#pragma unroll
  for (int bb = 0; bb < B; ++bb) { a0[bb] = 0.f; a1[bb] = 0.f; }
#pragma unroll 4
  for (int i = 0; i < 20; ++i) {
    const int row = rg + 32 * i;
    const unsigned wv = *(const unsigned*)(wm + (size_t)row * F + blockIdx.x * 16 + 2 * cp);
#pragma unroll
    for (int bb = 0; bb < B; ++bb) { const float hh = hn[bb * D + row]; a0[bb] = fmaf(hh, bf_lo(wv), a0[bb]); a1[bb] = fmaf(hh, bf_hi(wv), a1[bb]); }
  }
#pragma unroll
  for (int bb = 0; bb < B; ++bb) {
    a0[bb] += __shfl_xor(a0[bb], 16); a0[bb] += __shfl_xor(a0[bb], 32);
    a1[bb] += __shfl_xor(a1[bb], 16); a1[bb] += __shfl_xor(a1[bb], 32);
  }
  if (lane < 16) {
#pragma unroll
    for (int bb = 0; bb < B; ++bb) { red[w][which][bb][2 * cp] = a0[bb]; red[w][which][bb][2 * cp + 1] = a1[bb]; }
  }
  __syncthreads();
  if (tid < 128) {
    const int bb = tid >> 4, cc = tid & 15;
    float g = 0.f, u = 0.f;
#pragma unroll
    for (int ww = 0; ww < 8; ++ww) { g += red[ww][0][bb][cc]; u += red[ww][1][bb][cc]; }
    g = rbf(g); u = rbf(u);
    const float inner = 0.7978845608028654f * (g + 0.044715f * g * g * g);
    const float ge = rbf(0.5f * g * (1.f + tanhf(inner)));
    act[bb * F + blockIdx.x * 16 + cc] = rbf(ge * u);
  }
}

__global__ void __launch_bounds__(512) k_down(const float* __restrict__ act, const float* __restrict__ hbuf,
                                              const unsigned short* __restrict__ wd, unsigned short* __restrict__ out) {
  __shared__ __attribute__((aligned(16))) float sa[B * F];
  __shared__ __attribute__((aligned(16))) float red[8][B][16];
  const int tid = threadIdx.x, lane = tid & 63, w = tid >> 6;
  for (int i = tid; i < B * F / 4; i += 512) ((f32x4*)sa)[i] = ((const f32x4*)act)[i];
  __syncthreads();
  const int cp = tid & 7, rg = tid >> 3;
  float a0[B], a1[B];
#pragma unroll
  for (int bb = 0; bb < B; ++bb) { a0[bb] = 0.f; a1[bb] = 0.f; }
#pragma unroll 9
  for (int i = 0; i < 27; ++i) {
    const int row = rg + 64 * i;
    const unsigned wv = *(const unsigned*)(wd + (size_t)row * D + blockIdx.x * 16 + 2 * cp);
#pragma unroll
    for (int bb = 0; bb < B; ++bb) { const float hh = sa[bb * F + row]; a0[bb] = fmaf(hh, bf_lo(wv), a0[bb]); a1[bb] = fmaf(hh, bf_hi(wv), a1[bb]); }
  }
#pragma unroll
  for (int bb = 0; bb < B; ++bb) {
    a0[bb] += __shfl_xor(a0[bb], 8); a0[bb] += __shfl_xor(a0[bb], 16); a0[bb] += __shfl_xor(a0[bb], 32);
    a1[bb] += __shfl_xor(a1[bb], 8); a1[bb] += __shfl_xor(a1[bb], 16); a1[bb] += __shfl_xor(a1[bb], 32);
  }
  if (lane < 8) {
#pragma unroll
    for (int bb = 0; bb < B; ++bb) { red[w][bb][2 * cp] = a0[bb]; red[w][bb][2 * cp + 1] = a1[bb]; }
  }
  __syncthreads();
  if (tid < 128) {
    const int bb = tid >> 4, cc = tid & 15;
    float s = 0.f;
#pragma unroll
    for (int ww = 0; ww < 8; ++ww) s += red[ww][bb][cc];
    const int col = blockIdx.x * 16 + cc;
    out[bb * D + col] = f2bf(hbuf[bb * D + col] + rbf(s));
  }
}
}

extern "C" void kernel_launch(void* const* d_in, const int* in_sizes, int n_in, void* d_out, int out_size, void* d_ws,
                              size_t ws_size, hipStream_t stream) {
  const unsigned short* x = (const unsigned short*)d_in[0];
  const unsigned short* kc = (const unsigned short*)d_in[1];
  const unsigned short* vc = (const unsigned short*)d_in[2];
  const unsigned short* wq = (const unsigned short*)d_in[3];
  const unsigned short* wo = (const unsigned short*)d_in[4];
  const unsigned short* wg = (const unsigned short*)d_in[6];
  const unsigned short* wu = (const unsigned short*)d_in[7];
  const unsigned short* wd = (const unsigned short*)d_in[8];
  float* part = (float*)d_ws;
  float* hbuf = part + B * H * D;
  float* act = hbuf + B * D;
  k_attn<<<B * H, NT, 0, stream>>>(x, kc, vc, wq, wo, part);
  k_gateup<<<F / 16, 512, 0, stream>>>(x, part, wg, wu, hbuf, act);
  k_down<<<D / 16, 512, 0, stream>>>(act, hbuf, wd, (unsigned short*)d_out);
}
```

```cpp
#define STAMPS 1
#include <cstdio>
#include <hip/hip_runtime.h>
#include <hip/hip_bf16.h>
#include <stdint.h>

#define RDEPTH 5
#define W_AUX 0
#define QPOLL_SLEEP 4
#define NO_W 0
#define PKV 0
#define DELAY_A 18
#define DELAY_B 12
#define DELAY_C 20
#define HSLEEP 26
#define POLL_SLEEP 0
#define POLL_SLEEP_FAR 12
#define POLL_FAR 16

namespace {
constexpr int B = 8, D = 640, H = 32, SK = 4096, DH = 128, F = 1728;
typedef unsigned u32x4 __attribute__((ext_vector_type(4)));
typedef unsigned u32x2 __attribute__((ext_vector_type(2)));
typedef float f32x4 __attribute__((ext_vector_type(4)));
typedef float f32x2 __attribute__((ext_vector_type(2)));
typedef short s16x4 __attribute__((ext_vector_type(4)));
typedef __bf16 bf16x2 __attribute__((ext_vector_type(2)));
typedef __bf16 bf16x8 __attribute__((ext_vector_type(8)));
#define LAS __attribute__((address_space(3)))
#define GAS __attribute__((address_space(1)))
typedef __amdgpu_buffer_rsrc_t rsrc_t;

__device__ __forceinline__ float bf_lo(unsigned u) { return __builtin_bit_cast(float, u << 16); }
__device__ __forceinline__ float bf_hi(unsigned u) { return __builtin_bit_cast(float, u & 0xffff0000u); }
__device__ __forceinline__ float bf2f(unsigned short u) { return __builtin_bit_cast(float, (unsigned)u << 16); }
__device__ __forceinline__ unsigned short f2bf(float f) { return __builtin_bit_cast(unsigned short, (__bf16)f); }
__device__ __forceinline__ float rbf(float f) { return bf2f(f2bf(f)); }
__device__ __forceinline__ unsigned pk2(float lo, float hi) {
  bf16x2 v = {(__bf16)lo, (__bf16)hi};
  return __builtin_bit_cast(unsigned, v);
}
__device__ __forceinline__ float u2f(unsigned u) { return __builtin_bit_cast(float, u); }
__device__ __forceinline__ unsigned f2u(float f) { return __builtin_bit_cast(unsigned, f); }
__device__ __forceinline__ float dot2(unsigned k, unsigned q, float acc) {
  return __builtin_amdgcn_fdot2_f32_bf16(__builtin_bit_cast(bf16x2, k), __builtin_bit_cast(bf16x2, q), acc, false);
}
__device__ __forceinline__ f32x4 mfma(u32x4 a, u32x4 b, f32x4 c) {
  return __builtin_amdgcn_mfma_f32_16x16x32_bf16(__builtin_bit_cast(bf16x8, a), __builtin_bit_cast(bf16x8, b), c, 0, 0, 0);
}
template <int CTRL> __device__ __forceinline__ float dpp(float x) {
  return __builtin_bit_cast(float, __builtin_amdgcn_mov_dpp(__builtin_bit_cast(int, x), CTRL, 0xf, 0xf, true));
}
template <int CTRL> __device__ __forceinline__ unsigned dppu(unsigned x) { return (unsigned)__builtin_amdgcn_mov_dpp((int)x, CTRL, 0xf, 0xf, true); }
constexpr int XOR1 = 0xB1, XOR2 = 0x4E, XOR7 = 0x141, XOR8 = 0x128, BCAST = 0x150;
__device__ __forceinline__ float row16_sum(float x) { x += dpp<XOR1>(x); x += dpp<XOR2>(x); x += dpp<XOR7>(x); x += dpp<XOR8>(x); return x; }
__device__ __forceinline__ float row16_max(float x) {
  x = fmaxf(x, dpp<XOR1>(x)); x = fmaxf(x, dpp<XOR2>(x)); x = fmaxf(x, dpp<XOR7>(x)); x = fmaxf(x, dpp<XOR8>(x)); return x;
}
__device__ __forceinline__ float swap16_add(float a, float b) {
  auto s = __builtin_amdgcn_permlane16_swap(f2u(a), f2u(b), false, false);
  return u2f(s[0]) + u2f(s[1]);
}
__device__ __forceinline__ float swap32_add(float a, float b) {
  auto s = __builtin_amdgcn_permlane32_swap(f2u(a), f2u(b), false, false);
  return u2f(s[0]) + u2f(s[1]);
}
__device__ __forceinline__ float xrow_sum(float x) { x = swap16_add(x, x); return swap32_add(x, x); }
__device__ __forceinline__ float xrow_max(float x) {
  auto s = __builtin_amdgcn_permlane16_swap(f2u(x), f2u(x), false, false);
  x = fmaxf(u2f(s[0]), u2f(s[1]));
  auto t = __builtin_amdgcn_permlane32_swap(f2u(x), f2u(x), false, false);
  return fmaxf(u2f(t[0]), u2f(t[1]));
}
__device__ __forceinline__ float wave_sum(float x) { return xrow_sum(row16_sum(x)); }
constexpr int bitrev4(int j) { return ((j & 1) << 3) | ((j & 2) << 1) | ((j & 4) >> 1) | ((j & 8) >> 3); }

#define SB() __builtin_amdgcn_sched_barrier(0)
#define CBAR() asm volatile("" ::: "memory")
#define WG_BARRIER() do { asm volatile("s_waitcnt lgkmcnt(0)" ::: "memory"); __builtin_amdgcn_s_barrier(); CBAR(); } while (0)
#define PSTAMP 0
#define VSTAMP 0
#define VST(i)
#define TSTAMP 0
#define TST(i)
#define STAMP(i) st[i] = __builtin_amdgcn_s_memrealtime()
#define PST(i)

constexpr int R = RDEPTH;
constexpr int NT = 2, SC1 = 16;
constexpr int RS3 = 0x00020000;
constexpr int OOB = 0x7ffffff0;
constexpr float SCALE = 0.08838834764831845f, LOG2E = 1.4426950408889634f;

constexpr int WS_SEED = 0;
constexpr int WS_QP = 4096;
constexpr int WS_PA = WS_QP + 32 * 8 * 8 * 1024;
constexpr int WS_HB = WS_QP, HB_STRIDE = 8192;
constexpr int WS_PC = WS_PA;
constexpr int WS_END = WS_PA + 8 * 32 * 32 * 128;
constexpr int WS_STAMPS = 16 << 20;
constexpr int L_G = 0, L_U = 640 * 112, L_RB = 2 * 640 * 112;
constexpr int L_PA = L_RB + 4 * 640 * 4;
constexpr int L_PML = L_PA + 4 * 128 * 4;
constexpr int L_Q = L_PML + 32;
constexpr int L_TAG = L_Q + 256;
constexpr int L_HN = L_TAG + 16;
constexpr int L_ACT = L_HN + 1280;
constexpr int L_END = L_ACT + 128;
static_assert(L_END <= 163840, "LDS");

__device__ __forceinline__ void to_frags(const u32x4 (&raw)[8], u32x4* fr) {
#pragma unroll
  for (int d = 0; d < 4; ++d) {
    u32x4 lo, hi;
#pragma unroll
    for (int kp = 0; kp < 4; ++kp) {
      lo[kp] = __builtin_amdgcn_perm(raw[2 * kp + 1][d], raw[2 * kp][d], 0x05040100u);
      hi[kp] = __builtin_amdgcn_perm(raw[2 * kp + 1][d], raw[2 * kp][d], 0x07060302u);
    }
    fr[2 * d] = lo; fr[2 * d + 1] = hi;
  }
}

template <int N, bool FAR, bool TWO = false> __device__ __forceinline__ void sweep(rsrc_t rs, int voff, unsigned tag, u32x4 (&v)[N]) {
  for (int spin = 0; spin < (1 << 20); ++spin) {
#pragma unroll
    for (int i = 0; i < N; ++i) v[i] = __builtin_amdgcn_raw_buffer_load_b128(rs, voff + (i & 3) * 1024, (i >> 2) * 4096, SC1);
    unsigned bad = 0u;
#pragma unroll
    for (int i = 0; i < N; ++i) { bad |= v[i][3] ^ tag; if (TWO) bad |= v[i][1] ^ tag; }
    const unsigned long long m = __builtin_amdgcn_ballot_w64(bad != 0u);
    if (m == 0ull) break;
    if (FAR && __builtin_popcountll(m) > POLL_FAR) __builtin_amdgcn_s_sleep(POLL_SLEEP_FAR);
    else if (POLL_SLEEP) __builtin_amdgcn_s_sleep(POLL_SLEEP);
    CBAR();
  }
}

struct Stream {
  rsrc_t rk, rv, rd;
  int voff, doff, doff2;
  u32x4 ring[32];
  u32x4 draw[3][8];
  unsigned q[4];
  float x[16];
  float sc[16];
  float m, l, acc[8];
  int c;
  unsigned long long* st;

  template <int J, int PH> __device__ __forceinline__ void issue(int n0) {
    if constexpr (PH == 0 || (PH == 1 && J < 32)) ring[J & 31] = __builtin_amdgcn_raw_buffer_load_b128(rk, voff, (n0 + J) * 4096, NT);
    else ring[J & 31] = __builtin_amdgcn_raw_buffer_load_b128(rv, voff, (n0 + J - 256) * 4096, NT);
  }
  template <int CTRL, int BIT> __device__ __forceinline__ float tr(float lo, float hi) {
    const float t1 = lo + dpp<CTRL>(lo), t2 = hi + dpp<CTRL>(hi);
    return (c & BIT) ? t2 : t1;
  }
  template <int J> __device__ __forceinline__ void consume_k() {
    constexpr int I = J & 15;
    float d = dot2(ring[J][0], q[0], 0.f);
    d = dot2(ring[J][1], q[1], d); d = dot2(ring[J][2], q[2], d); d = dot2(ring[J][3], q[3], d);
    x[I] = d;
    if constexpr ((I & 1) == 1) x[I] = tr<XOR8, 8>(x[I - 1], x[I]);
    if constexpr ((I & 3) == 3) x[I] = tr<XOR7, 4>(x[I - 2], x[I]);
    if constexpr ((I & 7) == 7) x[I] = tr<XOR2, 2>(x[I - 4], x[I]);
    if constexpr (I == 15) x[I] = rbf(rbf(tr<XOR1, 1>(x[I - 8], x[I])) * SCALE);
    asm volatile("" : "+v"(x[I]));
    if constexpr (J == 15) sc[14] = x[15];
    if constexpr (J == 31) sc[15] = x[15];
  }
  template <int J> __device__ __forceinline__ void consume_v() {
    const float pb = dpp<BCAST + bitrev4(J & 15)>(sc[J >> 4]);
    f32x2 t[4];
#pragma unroll
    for (int k = 0; k < 4; ++k) t[k] = __builtin_elementwise_fma(f32x2{pb, pb}, f32x2{bf_lo(ring[J][k]), bf_hi(ring[J][k])}, f32x2{acc[2 * k], acc[2 * k + 1]});
    asm volatile("" : "+v"(t[0]), "+v"(t[1]), "+v"(t[2]), "+v"(t[3]));
#pragma unroll
    for (int k = 0; k < 4; ++k) { acc[2 * k] = t[k][0]; acc[2 * k + 1] = t[k][1]; }
  }
  __device__ __forceinline__ void rotate() {
#pragma unroll
    for (int k = 0; k < 14; ++k) sc[k] = sc[k + 2];
  }
  __device__ __forceinline__ void softmax() {
    float mx = sc[0];
#pragma unroll
    for (int k = 1; k < 16; ++k) mx = fmaxf(mx, sc[k]);
    m = xrow_max(row16_max(mx));
    l = 0.f;
#pragma unroll
    for (int k = 0; k < 16; ++k) { sc[k] = __builtin_amdgcn_exp2f((sc[k] - m) * LOG2E); l += sc[k]; }
  }
  template <int J> __device__ __forceinline__ void fill() {
    if constexpr (J < 32) {
      if constexpr (J >= R) asm volatile("s_waitcnt vmcnt(%0)" :: "n"(R - 1) : "memory");
      if constexpr (J == R || J == 8 || J == 16 || J == 24 || J == 31) PST(J == R ? 1 : J == 31 ? 5 : J / 8 + 1);
      issue<J, 0>(0);
      SB();
      fill<J + 1>();
    }
  }
  template <int J> __device__ __forceinline__ void first() {
    if constexpr (J < 32) {
      consume_k<J>();
      if constexpr (J & 1) {
        asm volatile("s_waitcnt vmcnt(%0)" :: "n"(R - 1) : "memory");
        issue<32 + J / 2, 0>(0);
      }
      SB();
      first<J + 1>();
    }
  }
  static constexpr int NI2 = 16 + R;
  static constexpr bool issues(int J) { return (J + 1) * NI2 / 32 > J * NI2 / 32; }
  static constexpr int pace2(int J) {
    int kv = 0, n = 0;
    for (int t = J - 1; t >= 0; --t) {
      if (t < 24) ++n;
      if (issues(t)) { if (++kv == R) return n; ++n; }
    }
    return n + (R - 1 - kv);
  }
  template <int J> __device__ __forceinline__ void second() {
    if constexpr (J < 32) {
      if constexpr (issues(J)) {
        asm volatile("s_waitcnt vmcnt(%0)" :: "n"(pace2(J)) : "memory");
        issue<16 + J * NI2 / 32, 0>(32);
        SB();
      }
      if constexpr (J < 24) draw[J / 8][J % 8] = __builtin_amdgcn_raw_buffer_load_b128(rd, J < 16 ? doff : doff2, (J % 8) * (D * 2) + (J / 8) * 256, W_AUX);
      SB();
      consume_k<J>();
      SB();
      second<J + 1>();
    }
  }
  template <int J, int PH> __device__ __forceinline__ void steady(int n0) {
    if constexpr (J < 32) {
      asm volatile("s_waitcnt vmcnt(%0)" :: "n"(R - 1) : "memory");
      issue<J + R, PH>(n0);
      SB();
      if constexpr (PH < 2) consume_k<J>(); else consume_v<J>();
      SB();
      steady<J + 1, PH>(n0);
    }
  }
};

struct Args {
  const unsigned short *x, *kc, *vc, *wq, *wo, *wg, *wu, *wd;
  unsigned short* out;
  unsigned char* ws;
};

__device__ __forceinline__ void gu_frags(LAS unsigned char* L, int tile, int lane, u32x4 (&gf)[20]) {
  const int g = lane >> 4, q = (lane >> 2) & 3, p = lane & 3;
  LAS unsigned char* a = L + (p < 2 ? L_G : L_U) + (8 * g + q) * 112 + tile * 16 + (p & 1) * 8;
#pragma unroll
  for (int s = 0; s < 20; ++s) {
    const s16x4 t0 = __builtin_amdgcn_ds_read_tr16_b64_v4i16((LAS s16x4*)(a + s * 3584));
    const s16x4 t1 = __builtin_amdgcn_ds_read_tr16_b64_v4i16((LAS s16x4*)(a + s * 3584 + 448));
    const u32x2 u0 = __builtin_bit_cast(u32x2, t0), u1 = __builtin_bit_cast(u32x2, t1);
    gf[s] = u32x4{u0[0], u0[1], u1[0], u1[1]};
  }
}
__device__ __forceinline__ void gu_tile(LAS unsigned char* L, int tile, int lane, const u32x4 (&gf)[20]) {
  f32x4 acc = {0.f, 0.f, 0.f, 0.f};
  const LAS u32x4* hn = (const LAS u32x4*)(L + L_HN) + (lane >> 4);
#pragma unroll
  for (int s = 0; s < 20; ++s) acc = mfma(hn[4 * s], gf[s], acc);
  const float g = rbf(acc[0]), u = rbf(dpp<XOR8>(acc[0]));
  const float z = 0.7978845608028654f * (g + 0.044715f * g * g * g);
  const float e = __builtin_amdgcn_exp2f(z * (2.f * LOG2E));
  const float th = 1.f - 2.f * __builtin_amdgcn_rcpf(e + 1.f);
  const float ge = rbf(0.5f * g * (1.f + th));
  if (lane < 8) ((LAS unsigned short*)(L + L_ACT))[tile * 8 + lane] = f2bf(ge * u);
}
template <int NB> __device__ __forceinline__ void gemv_blocks(const u32x4 af, const u32x4* fr, LAS unsigned char* dst, int lane) {
  u32x4 am[8];
#pragma unroll
  for (int e = 0; e < 8; ++e)
#pragma unroll
    for (int k = 0; k < 4; ++k) am[e][k] = (lane & 7) == e ? af[k] : 0u;
  LAS f32x4* o = (LAS f32x4*)(dst + (lane & 15) * 32 + ((lane >> 4) & 1) * 16);
#pragma unroll
  for (int n = 0; n < NB; ++n) {
    f32x4 d = {0.f, 0.f, 0.f, 0.f};
#pragma unroll
    for (int e = 0; e < 8; ++e) d = mfma(am[e], fr[n * 8 + e], d);
    o[n * 32] = d;
  }
}
template <int NP> __device__ __forceinline__ void publish_cols(LAS unsigned char* L, rsrc_t rw, int base, int b, int h, int t, unsigned tag) {
  const int j = t >> 3, u = t & 7;
  const LAS float* p = (const LAS float*)(L + L_RB) + 20 * j + 3 * u;
  float s0 = p[0], s1 = p[1], s2 = p[2];
#pragma unroll
  for (int k = 1; k < NP; ++k) { s0 += p[k * 640]; s1 += p[k * 640 + 1]; s2 += p[k * 640 + 2]; }
  const u32x4 v = {u < 7 ? f2u(s0) : 0u, u < 7 ? f2u(s1) : 0u, u < 6 ? f2u(s2) : 0u, tag};
  __builtin_amdgcn_raw_buffer_store_b128(v, rw, base + ((b * 32 + j) * 32 + h) * 128 + u * 16, 0, SC1);
}
template <bool FAR> __device__ __forceinline__ void gather_cols(rsrc_t rw, int base, int b, int j, int lane, unsigned tag, float (&s)[3]) {
  u32x4 v[4];
  sweep<4, FAR>(rw, base + (b * 32 + j) * 4096 + lane * 16, tag, v);
#pragma unroll
  for (int e = 0; e < 3; ++e) {
    float x = (u2f(v[0][e]) + u2f(v[1][e])) + (u2f(v[2][e]) + u2f(v[3][e]));
    x += dpp<XOR8>(x);
    s[e] = xrow_sum(x);
  }
}

__global__ void __launch_bounds__(512, 2) k_block(Args a) {
  __shared__ __attribute__((aligned(16))) unsigned char lds_[L_END];
  LAS unsigned char* L = (LAS unsigned char*)lds_;
  unsigned long long st[8] = {0, 0, 0, 0, 0, 0, 0, 0};
  STAMP(0); PST(0); VST(0);
  const int bid = blockIdx.x, h = bid & 31, b = bid >> 5;
  const int tid = threadIdx.x, lane = tid & 63, w = __builtin_amdgcn_readfirstlane(tid >> 6), r = lane >> 4, c = lane & 15;
  const int ch0 = 27 * (h & 7) + 7 * (h >> 3), nch = (h >> 3) == 3 ? 6 : 7;
  const rsrc_t rw = __builtin_amdgcn_make_buffer_rsrc((void*)a.ws, 0, WS_END, RS3);

  if (w < 4) {
    __builtin_amdgcn_s_setprio(2);
    Stream s;
    s.rk = __builtin_amdgcn_make_buffer_rsrc((void*)(a.kc + (size_t)bid * SK * DH), 0, SK * DH * 2, RS3);
    s.rv = __builtin_amdgcn_make_buffer_rsrc((void*)(a.vc + (size_t)bid * SK * DH), 0, SK * DH * 2, RS3);
    s.rd = __builtin_amdgcn_make_buffer_rsrc((void*)(a.wd + (size_t)ch0 * 8 * D), 0, NO_W ? 0 : nch * 8 * D * 2, RS3);
    s.voff = w * 1024 + lane * 16;
    const int kb = w & 1, nb0 = (w >> 1) * 3;
    s.doff = (32 * kb + 8 * r) * (D * 2) + nb0 * 256 + c * 16;
    s.doff2 = w < 2 ? s.doff : OOB - 65536;
    s.c = c;
#pragma unroll
    for (int k = 0; k < 8; ++k) s.acc[k] = 0.f;
#pragma unroll
    for (int k = 0; k < 16; ++k) s.sc[k] = 0.f;
    s.st = st;
    s.fill<0>();
    STAMP(1);
    WG_BARRIER();
    {
      const u32x4 qq = *(const LAS u32x4*)(L + L_Q + c * 16);
#pragma unroll
      for (int k = 0; k < 4; ++k) s.q[k] = qq[k];
    }
    const unsigned tag = __builtin_amdgcn_readfirstlane(*(const LAS unsigned*)(L + L_TAG));
    SB();
    PST(6); VST(1);
    s.first<0>();
    s.rotate();
    s.second<0>();
    u32x4 df[24];
    to_frags(s.draw[0], df); to_frags(s.draw[1], df + 8); to_frags(s.draw[2], df + 16);
#pragma unroll
    for (int k = 0; k < 24; ++k) asm volatile("" : "+v"(df[k]));
    SB();
    for (int n0 = 64; n0 < 224; n0 += 32) { if (n0 == 128) VST(2); s.rotate(); s.steady<0, 0>(n0); }
    s.rotate();
    s.steady<0, 1>(224);
    VST(3);
    s.softmax();
    SB();
    for (int n0 = 256; n0 < 512; n0 += 32) { if (n0 == 320) VST(4); if (n0 == 384) VST(5); if (n0 == 448) VST(6); s.steady<0, 2>(n0); s.rotate(); }
    VST(7);
    {
      const float a0 = swap16_add(s.acc[0], s.acc[1]), a1 = swap16_add(s.acc[2], s.acc[3]);
      const float a2 = swap16_add(s.acc[4], s.acc[5]), a3 = swap16_add(s.acc[6], s.acc[7]);
      const float b0 = swap32_add(a0, a1), b1 = swap32_add(a2, a3);
      const float Ls = wave_sum(s.l);
      LAS float* pa = (LAS float*)(L + L_PA) + w * 128 + 8 * c + r;
      pa[0] = b0; pa[4] = b1;
      if (lane == 0) { ((LAS float*)(L + L_PML))[w] = s.m; ((LAS float*)(L + L_PML))[4 + w] = Ls; }
    }
    STAMP(2);
    WG_BARRIER();
    u32x4 gf[20];
    float hres[3] = {0.f, 0.f, 0.f};
    const int u = lane & 7;
    if (w != 0) gu_frags(L, 3 + w, lane, gf);
    else {
#pragma unroll
      for (int k = 0; k < 20; ++k) gf[k] = u32x4{0u, 0u, 0u, 0u};
      const rsrc_t rx = __builtin_amdgcn_make_buffer_rsrc((void*)(a.x + b * D + 20 * h), 0, 40, RS3);
#pragma unroll
      for (int e = 0; e < 3; ++e) hres[e] = bf2f(__builtin_amdgcn_raw_buffer_load_b16(rx, (3 * u + e) * 2, 0, 0));
    }
    WG_BARRIER();
    STAMP(3); TST(0);
    if (w == 0) {
      float sa[3];
      if (DELAY_A) __builtin_amdgcn_s_sleep(DELAY_A);
      gather_cols<true>(rw, WS_PA, b, h, lane, tag, sa);
      STAMP(4); TST(1);
#pragma unroll
      for (int e = 0; e < 3; ++e) hres[e] = rbf(hres[e] + rbf(sa[e]));
      __builtin_amdgcn_raw_buffer_store_b128(u32x4{f2u(hres[0]), f2u(hres[1]), f2u(hres[2]), tag + 1u}, rw,
                                             lane < 8 ? WS_HB + b * HB_STRIDE + h * 128 + lane * 16 : OOB, 0, SC1);
      if (DELAY_B) __builtin_amdgcn_s_sleep(DELAY_B);
      TST(2);
      u32x4 v[4];
      sweep<4, false>(rw, WS_HB + b * HB_STRIDE + lane * 16, tag + 1u, v);
      STAMP(5); TST(3);
      float sum = 0.f;
#pragma unroll
      for (int i = 0; i < 4; ++i) sum += (u2f(v[i][0]) + u2f(v[i][1])) + u2f(v[i][2]);
      const float mean = wave_sum(sum) * (1.f / D);
      float v01 = 0.f, v2 = 0.f;
#pragma unroll
      for (int i = 0; i < 4; ++i) {
        const float d0 = u2f(v[i][0]) - mean, d1 = u2f(v[i][1]) - mean, d2 = u2f(v[i][2]) - mean;
        v01 = fmaf(d0, d0, v01); v01 = fmaf(d1, d1, v01); v2 = fmaf(d2, d2, v2);
      }
      const float var = wave_sum((u < 7 ? v01 : 0.f) + (u < 6 ? v2 : 0.f)) * (1.f / D);
      const float mb = rbf(mean), ri = __builtin_amdgcn_rcpf(rbf(__builtin_amdgcn_sqrtf(rbf(rbf(var) + 1e-5f))));
      LAS unsigned short* hn = (LAS unsigned short*)(L + L_HN) + 20 * (lane >> 3) + 3 * u;
      LAS unsigned short* dump = (LAS unsigned short*)(L + L_PA);
      LAS unsigned short* h01 = u < 7 ? hn : dump;
      LAS unsigned short* h2 = u < 6 ? hn : dump;
#pragma unroll
      for (int i = 0; i < 4; ++i) {
        h01[160 * i] = f2bf(rbf(u2f(v[i][0]) - mb) * ri);
        h01[160 * i + 1] = f2bf(rbf(u2f(v[i][1]) - mb) * ri);
        h2[160 * i + 2] = f2bf(rbf(u2f(v[i][2]) - mb) * ri);
      }
      TST(4);
    }
    WG_BARRIER();
    if (w != 0) gu_tile(L, 3 + w, lane, gf);
    WG_BARRIER();
    TST(5);
    {
      const u32x4 af = *(const LAS u32x4*)(L + L_ACT + kb * 64 + r * 16);
      if (w < 2) gemv_blocks<3>(af, df, L + L_RB + kb * 2560, lane);
      else gemv_blocks<2>(af, df, L + L_RB + kb * 2560 + 3 * 512, lane);
    }
    TST(6);
    WG_BARRIER();
    STAMP(6);
    if (w == 0) {
      float sc[3];
      if (DELAY_C) __builtin_amdgcn_s_sleep(DELAY_C);
      gather_cols<false>(rw, WS_PC, b, h, lane, tag + 1u, sc);
      TST(7);
      const rsrc_t ry = __builtin_amdgcn_make_buffer_rsrc((void*)(a.out + b * D + 20 * h), 0, 40, RS3);
#pragma unroll
      for (int e = 0; e < 3; ++e) __builtin_amdgcn_raw_buffer_store_b16(f2bf(hres[e] + rbf(sc[e])), ry, lane < 8 ? (3 * u + e) * 2 : OOB, 0, 0);
      if (bid == 0 && lane == 0) __hip_atomic_store((GAS unsigned*)(a.ws + WS_SEED), tag + 1u, __ATOMIC_RELAXED, __HIP_MEMORY_SCOPE_AGENT);
    }
  } else {
    const int hw = w - 4;
    if (hw == 0) {
      const int hq = bid >> 3, ks = bid & 7;
      const unsigned seed = __builtin_amdgcn_raw_buffer_load_b32(rw, WS_SEED, 0, SC1);
      const rsrc_t rx = __builtin_amdgcn_make_buffer_rsrc((void*)a.x, 0, B * D * 2, RS3);
      const rsrc_t rq = __builtin_amdgcn_make_buffer_rsrc((void*)(a.wq + hq * DH), 0, (80 * (ks + 1) - 1) * (H * DH * 2) + DH * 2, RS3);
      u32x4 xs[10], xa[3], raw[3][8];
      const int xrow = (c & 7) * (D * 2);
#pragma unroll
      for (int i = 0; i < 10; ++i) xs[i] = __builtin_amdgcn_raw_buffer_load_b128(rx, (lane >> 3) * (D * 2) + (lane & 7) * 16 + i * 128, 0, 0);
#pragma unroll
      for (int k = 0; k < 3; ++k) xa[k] = __builtin_amdgcn_raw_buffer_load_b128(rx, (32 * k + 8 * r < 80) ? xrow + (80 * ks + 32 * k + 8 * r) * 2 : OOB, 0, 0);
      SB();
#pragma unroll
      for (int k = 0; k < 3; ++k)
#pragma unroll
        for (int i = 0; i < 8; ++i)
          raw[k][i] = __builtin_amdgcn_raw_buffer_load_b128(rq, 8 * r * (H * DH * 2) + c * 16, (80 * ks + 32 * k + i) * (H * DH * 2), W_AUX);
      SB();
      PST(1);
      float sum = 0.f, ssq = 0.f;
#pragma unroll
      for (int i = 0; i < 10; ++i)
#pragma unroll
        for (int k = 0; k < 4; ++k) { sum = dot2(xs[i][k], 0x3f803f80u, sum); ssq = dot2(xs[i][k], xs[i][k], ssq); }
      sum += dpp<XOR1>(sum); ssq += dpp<XOR1>(ssq); sum += dpp<XOR2>(sum); ssq += dpp<XOR2>(ssq); sum += dpp<XOR7>(sum); ssq += dpp<XOR7>(ssq);
      const float mean = sum * (1.f / D);
      const float var = fmaxf(fmaf(-mean, mean, ssq * (1.f / D)), 0.f);
      PST(2);
      const float mb = u2f(__builtin_amdgcn_ds_bpermute((c & 7) * 32, f2u(rbf(mean))));
      const float ri = u2f(__builtin_amdgcn_ds_bpermute((c & 7) * 32, f2u(__builtin_amdgcn_rcpf(rbf(__builtin_amdgcn_sqrtf(rbf(rbf(var) + 1e-5f)))))));
      PST(3);
      f32x4 d[8];
#pragma unroll
      for (int e = 0; e < 8; ++e) d[e] = f32x4{0.f, 0.f, 0.f, 0.f};
#pragma unroll
      for (int k = 0; k < 3; ++k) {
        u32x4 af;
#pragma unroll
        for (int i = 0; i < 4; ++i) af[i] = (32 * k + 8 * r < 80) ? pk2(rbf(bf_lo(xa[k][i]) - mb) * ri, rbf(bf_hi(xa[k][i]) - mb) * ri) : 0u;
        u32x4 fr[8];
        to_frags(raw[k], fr);
#pragma unroll
        for (int e = 0; e < 8; ++e) d[e] = mfma(af, fr[e], d[e]);
      }
      PST(4);
      const unsigned tag = __builtin_amdgcn_readfirstlane(seed) + 1u;
      const bool up = r >= 2;
#pragma unroll
      for (int i = 0; i < 4; ++i) {
        const int off = WS_QP + ((hq * 8 + ((4 * r + i) & 7)) * 8 + ks) * 1024 + (c + (up ? 16 : 0)) * 16;
        const float e0 = up ? d[4][i] : d[0][i], e1 = up ? d[5][i] : d[1][i], e2 = up ? d[6][i] : d[2][i], e3 = up ? d[7][i] : d[3][i];
        __builtin_amdgcn_raw_buffer_store_b128(u32x4{f2u(e0), tag, f2u(e1), tag}, rw, off, 0, SC1);
        __builtin_amdgcn_raw_buffer_store_b128(u32x4{f2u(e2), tag, f2u(e3), tag}, rw, off + 512, 0, SC1);
      }
      STAMP(4); PST(5);
      for (int spin = 0; spin < (1 << 20); ++spin) {
        __builtin_amdgcn_s_sleep(QPOLL_SLEEP);
        const u32x4 t = __builtin_amdgcn_raw_buffer_load_b128(rw, WS_QP + (h * 8 + b) * 8192 + (lane >> 3) * 1024 + 896 + (lane & 7) * 16, 0, SC1);
        if (__builtin_amdgcn_ballot_w64(t[3] != tag) == 0ull) break;
        CBAR();
      }
      u32x4 v[8];
      sweep<8, false, true>(rw, WS_QP + (h * 8 + b) * 8192 + lane * 16, tag, v);
      PST(6);
      float q0 = 0.f, q1 = 0.f;
#pragma unroll
      for (int i = 0; i < 8; ++i) { q0 += u2f(v[i][0]); q1 += u2f(v[i][2]); }
      ((LAS unsigned*)(L + L_Q))[4 * c + 2 * (r & 1) + (r >> 1)] = pk2(q0, q1);
      if (lane == 0) *(LAS unsigned*)(L + L_TAG) = tag;
      if (lane < 4) ((LAS unsigned*)(L + L_ACT))[28 + lane] = 0u;
    }
    STAMP(1);
    WG_BARRIER();
    const unsigned tag = __builtin_amdgcn_readfirstlane(*(const LAS unsigned*)(L + L_TAG));
    u32x4 of[40];
    {
      const rsrc_t ro = __builtin_amdgcn_make_buffer_rsrc((void*)(a.wo + (size_t)h * DH * D), 0, NO_W ? 0 : DH * D * 2, RS3);
      const rsrc_t rg = __builtin_amdgcn_make_buffer_rsrc((void*)(hw < 2 ? a.wg : a.wu), 0, NO_W ? 0 : D * F * 2, RS3);
      const int ooff = (32 * hw + 8 * r) * (D * 2) + c * 16;
      int id = (hw & 1) * 35 * 64 + lane;
      int row = id / 7, pc = id - 7 * row;
      LAS unsigned char* dst = L + (hw < 2 ? L_G : L_U) + (hw & 1) * 35 * 1024;
#pragma unroll
      for (int n = 0; n < 5; ++n) {
        u32x4 raw[8];
#pragma unroll
        for (int i = 0; i < 8; ++i) {
          raw[i] = __builtin_amdgcn_raw_buffer_load_b128(ro, ooff + n * 256, i * (D * 2), W_AUX);
          CBAR(); __builtin_amdgcn_s_sleep(HSLEEP); CBAR();
        }
        to_frags(raw, of + 8 * n);
#pragma unroll
        for (int e = 0; e < 8; ++e) asm volatile("" : "+v"(of[8 * n + e]));
        for (int i = 0; i < 7; ++i) {
          __builtin_amdgcn_raw_ptr_buffer_load_lds(rg, (LAS void*)dst, 16, pc < nch ? row * (F * 2) + (ch0 + pc) * 16 : OOB, 0, 0, W_AUX);
          dst += 1024;
          row += 9; pc += 1;
          if (pc >= 7) { pc -= 7; row += 1; }
          CBAR(); __builtin_amdgcn_s_sleep(HSLEEP); CBAR();
        }
      }
    }
    asm volatile("s_waitcnt vmcnt(0)" ::: "memory");
    STAMP(2);
    WG_BARRIER();
    TST(0);
    {
      const f32x4 mm = *(const LAS f32x4*)(L + L_PML), ll = *(const LAS f32x4*)(L + L_PML + 16);
      f32x4 pv[4][2];
#pragma unroll
      for (int ww = 0; ww < 4; ++ww) {
        const LAS f32x4* p = (const LAS f32x4*)(L + L_PA + (ww * 128 + 32 * hw + 8 * r) * 4);
        pv[ww][0] = p[0]; pv[ww][1] = p[1];
      }
      const float M = fmaxf(fmaxf(mm[0], mm[1]), fmaxf(mm[2], mm[3]));
      float wt[4], Ls = 0.f;
#pragma unroll
      for (int ww = 0; ww < 4; ++ww) { wt[ww] = __builtin_amdgcn_exp2f((mm[ww] - M) * LOG2E); Ls = fmaf(wt[ww], ll[ww], Ls); }
      const float inv = __builtin_amdgcn_rcpf(Ls);
      f32x4 o0 = pv[0][0] * wt[0], o1 = pv[0][1] * wt[0];
#pragma unroll
      for (int ww = 1; ww < 4; ++ww) { o0 += pv[ww][0] * wt[ww]; o1 += pv[ww][1] * wt[ww]; }
      o0 *= inv; o1 *= inv;
      const u32x4 af = {pk2(o0[0], o0[1]), pk2(o0[2], o0[3]), pk2(o1[0], o1[1]), pk2(o1[2], o1[3])};
      TST(1);
      gemv_blocks<5>(af, of, L + L_RB + hw * 2560, lane);
      TST(2);
    }
    WG_BARRIER();
    TST(3);
    publish_cols<4>(L, rw, WS_PA, b, h, tid - 256, tag);
    STAMP(3); TST(4);
    u32x4 gf[20];
    gu_frags(L, hw, lane, gf);
    WG_BARRIER();
    TST(5);
    gu_tile(L, hw, lane, gf);
    STAMP(5); TST(6);
    WG_BARRIER();
    WG_BARRIER();
    publish_cols<2>(L, rw, WS_PC, b, h, tid - 256, tag + 1u);
    STAMP(6); TST(7);
  }
  STAMP(7); PST(7);
  if (lane == 0 && w == 0 && (bid % 9) == 0) printf("WG %d t0 %u q %d send %d end %d\n", bid, (unsigned)(st[0] & 0xffffff), (int)(st[1] - st[0]), (int)(st[2] - st[0]), (int)(st[7] - st[0]));
  if (lane == 0) {
    GAS unsigned long long* so = (GAS unsigned long long*)(a.ws + WS_STAMPS) + ((size_t)bid * 8 + w) * 8;
#pragma unroll
    for (int i = 0; i < 8; ++i) so[i] = st[i];
  }
}
}

extern "C" void kernel_launch(void* const* d_in, const int* in_sizes, int n_in, void* d_out, int out_size, void* d_ws,
                              size_t ws_size, hipStream_t stream) {
  Args a;
  a.x = (const unsigned short*)d_in[0];
  a.kc = (const unsigned short*)d_in[1];
  a.vc = (const unsigned short*)d_in[2];
  a.wq = (const unsigned short*)d_in[3];
  a.wo = (const unsigned short*)d_in[4];
  a.wg = (const unsigned short*)d_in[6];
  a.wu = (const unsigned short*)d_in[7];
  a.wd = (const unsigned short*)d_in[8];
  a.out = (unsigned short*)d_out;
  a.ws = (unsigned char*)d_ws;
  fprintf(stderr, "ptrs x %p k %p v %p wq %p wo %p wg %p wu %p wd %p out %p ws %p\n", a.x, a.kc, a.vc, a.wq, a.wo, a.wg, a.wu, a.wd, a.out, a.ws);
  k_block<<<B * H, 512, 0, stream>>>(a);
}
```

```cpp
#define STAMPS 1
#include <hip/hip_runtime.h>
#include <hip/hip_bf16.h>
#include <stdint.h>

#define RDEPTH 5
#define W_AUX 0
#define QPOLL_SLEEP 4
#define NO_W 0
#define PKV 0
#define DELAY_A 18
#define DELAY_B 12
#define DELAY_C 20
#define HSLEEP 26
#define POLL_SLEEP 0
#define POLL_SLEEP_FAR 12
#define POLL_FAR 16

namespace {
constexpr int B = 8, D = 640, H = 32, SK = 4096, DH = 128, F = 1728;
typedef unsigned u32x4 __attribute__((ext_vector_type(4)));
typedef unsigned u32x2 __attribute__((ext_vector_type(2)));
typedef float f32x4 __attribute__((ext_vector_type(4)));
typedef float f32x2 __attribute__((ext_vector_type(2)));
typedef short s16x4 __attribute__((ext_vector_type(4)));
typedef __bf16 bf16x2 __attribute__((ext_vector_type(2)));
typedef __bf16 bf16x8 __attribute__((ext_vector_type(8)));
#define LAS __attribute__((address_space(3)))
#define GAS __attribute__((address_space(1)))
typedef __amdgpu_buffer_rsrc_t rsrc_t;

__device__ __forceinline__ float bf_lo(unsigned u) { return __builtin_bit_cast(float, u << 16); }
__device__ __forceinline__ float bf_hi(unsigned u) { return __builtin_bit_cast(float, u & 0xffff0000u); }
__device__ __forceinline__ float bf2f(unsigned short u) { return __builtin_bit_cast(float, (unsigned)u << 16); }
__device__ __forceinline__ unsigned short f2bf(float f) { return __builtin_bit_cast(unsigned short, (__bf16)f); }
__device__ __forceinline__ float rbf(float f) { return bf2f(f2bf(f)); }
__device__ __forceinline__ unsigned pk2(float lo, float hi) {
  bf16x2 v = {(__bf16)lo, (__bf16)hi};
  return __builtin_bit_cast(unsigned, v);
}
__device__ __forceinline__ float u2f(unsigned u) { return __builtin_bit_cast(float, u); }
__device__ __forceinline__ unsigned f2u(float f) { return __builtin_bit_cast(unsigned, f); }
__device__ __forceinline__ float dot2(unsigned k, unsigned q, float acc) {
  return __builtin_amdgcn_fdot2_f32_bf16(__builtin_bit_cast(bf16x2, k), __builtin_bit_cast(bf16x2, q), acc, false);
}
__device__ __forceinline__ f32x4 mfma(u32x4 a, u32x4 b, f32x4 c) {
  return __builtin_amdgcn_mfma_f32_16x16x32_bf16(__builtin_bit_cast(bf16x8, a), __builtin_bit_cast(bf16x8, b), c, 0, 0, 0);
}
template <int CTRL> __device__ __forceinline__ float dpp(float x) {
  return __builtin_bit_cast(float, __builtin_amdgcn_mov_dpp(__builtin_bit_cast(int, x), CTRL, 0xf, 0xf, true));
}
template <int CTRL> __device__ __forceinline__ unsigned dppu(unsigned x) { return (unsigned)__builtin_amdgcn_mov_dpp((int)x, CTRL, 0xf, 0xf, true); }
constexpr int XOR1 = 0xB1, XOR2 = 0x4E, XOR7 = 0x141, XOR8 = 0x128, BCAST = 0x150;
__device__ __forceinline__ float row16_sum(float x) { x += dpp<XOR1>(x); x += dpp<XOR2>(x); x += dpp<XOR7>(x); x += dpp<XOR8>(x); return x; }
__device__ __forceinline__ float row16_max(float x) {
  x = fmaxf(x, dpp<XOR1>(x)); x = fmaxf(x, dpp<XOR2>(x)); x = fmaxf(x, dpp<XOR7>(x)); x = fmaxf(x, dpp<XOR8>(x)); return x;
}
__device__ __forceinline__ float swap16_add(float a, float b) {
  auto s = __builtin_amdgcn_permlane16_swap(f2u(a), f2u(b), false, false);
  return u2f(s[0]) + u2f(s[1]);
}
__device__ __forceinline__ float swap32_add(float a, float b) {
  auto s = __builtin_amdgcn_permlane32_swap(f2u(a), f2u(b), false, false);
  return u2f(s[0]) + u2f(s[1]);
}
__device__ __forceinline__ float xrow_sum(float x) { x = swap16_add(x, x); return swap32_add(x, x); }
__device__ __forceinline__ float xrow_max(float x) {
  auto s = __builtin_amdgcn_permlane16_swap(f2u(x), f2u(x), false, false);
  x = fmaxf(u2f(s[0]), u2f(s[1]));
  auto t = __builtin_amdgcn_permlane32_swap(f2u(x), f2u(x), false, false);
  return fmaxf(u2f(t[0]), u2f(t[1]));
}
__device__ __forceinline__ float wave_sum(float x) { return xrow_sum(row16_sum(x)); }
constexpr int bitrev4(int j) { return ((j & 1) << 3) | ((j & 2) << 1) | ((j & 4) >> 1) | ((j & 8) >> 3); }

#define SB() __builtin_amdgcn_sched_barrier(0)
#define CBAR() asm volatile("" ::: "memory")
#define WG_BARRIER() do { asm volatile("s_waitcnt lgkmcnt(0)" ::: "memory"); __builtin_amdgcn_s_barrier(); CBAR(); } while (0)
#define PSTAMP 0
#define VSTAMP 0
#define VST(i)
#define TSTAMP 0
#define TST(i)
#define STAMP(i) st[i] = __builtin_amdgcn_s_memrealtime()
#define PST(i)

constexpr int R = RDEPTH;
constexpr int NT = 2, SC1 = 16;
constexpr int RS3 = 0x00020000;
constexpr int OOB = 0x7ffffff0;
constexpr float SCALE = 0.08838834764831845f, LOG2E = 1.4426950408889634f;

constexpr int WS_SEED = 0;
constexpr int WS_QP = 4096;
constexpr int WS_PA = WS_QP + 32 * 8 * 8 * 1024;
constexpr int WS_HB = WS_QP, HB_STRIDE = 8192;
constexpr int WS_PC = WS_PA;
constexpr int WS_END = WS_PA + 8 * 32 * 32 * 128;
constexpr int WS_STAMPS = 16 << 20;
constexpr int L_G = 0, L_U = 640 * 112, L_RB = 2 * 640 * 112;
constexpr int L_PA = L_RB + 4 * 640 * 4;
constexpr int L_PML = L_PA + 4 * 128 * 4;
constexpr int L_Q = L_PML + 32;
constexpr int L_TAG = L_Q + 256;
constexpr int L_HN = L_TAG + 16;
constexpr int L_ACT = L_HN + 1280;
constexpr int L_END = L_ACT + 128;
static_assert(L_END <= 163840, "LDS");

__device__ __forceinline__ void to_frags(const u32x4 (&raw)[8], u32x4* fr) {
#pragma unroll
  for (int d = 0; d < 4; ++d) {
    u32x4 lo, hi;
#pragma unroll
    for (int kp = 0; kp < 4; ++kp) {
      lo[kp] = __builtin_amdgcn_perm(raw[2 * kp + 1][d], raw[2 * kp][d], 0x05040100u);
      hi[kp] = __builtin_amdgcn_perm(raw[2 * kp + 1][d], raw[2 * kp][d], 0x07060302u);
    }
    fr[2 * d] = lo; fr[2 * d + 1] = hi;
  }
}

template <int N, bool FAR, bool TWO = false> __device__ __forceinline__ void sweep(rsrc_t rs, int voff, unsigned tag, u32x4 (&v)[N]) {
  for (int spin = 0; spin < (1 << 20); ++spin) {
#pragma unroll
    for (int i = 0; i < N; ++i) v[i] = __builtin_amdgcn_raw_buffer_load_b128(rs, voff + (i & 3) * 1024, (i >> 2) * 4096, SC1);
    unsigned bad = 0u;
#pragma unroll
    for (int i = 0; i < N; ++i) { bad |= v[i][3] ^ tag; if (TWO) bad |= v[i][1] ^ tag; }
    const unsigned long long m = __builtin_amdgcn_ballot_w64(bad != 0u);
    if (m == 0ull) break;
    if (FAR && __builtin_popcountll(m) > POLL_FAR) __builtin_amdgcn_s_sleep(POLL_SLEEP_FAR);
    else if (POLL_SLEEP) __builtin_amdgcn_s_sleep(POLL_SLEEP);
    CBAR();
  }
}

struct Stream {
  rsrc_t rk, rv, rd;
  int voff, doff, doff2;
  u32x4 ring[32];
  u32x4 draw[3][8];
  unsigned q[4];
  float x[16];
  float sc[16];
  float m, l, acc[8];
  int c;
  unsigned long long* st;

  template <int J, int PH> __device__ __forceinline__ void issue(int n0) {
    if constexpr (PH == 0 || (PH == 1 && J < 32)) ring[J & 31] = __builtin_amdgcn_raw_buffer_load_b128(rk, voff, (n0 + J) * 4096, NT);
    else ring[J & 31] = __builtin_amdgcn_raw_buffer_load_b128(rv, voff, (n0 + J - 256) * 4096, NT);
  }
  template <int CTRL, int BIT> __device__ __forceinline__ float tr(float lo, float hi) {
    const float t1 = lo + dpp<CTRL>(lo), t2 = hi + dpp<CTRL>(hi);
    return (c & BIT) ? t2 : t1;
  }
  template <int J> __device__ __forceinline__ void consume_k() {
    constexpr int I = J & 15;
    float d = dot2(ring[J][0], q[0], 0.f);
    d = dot2(ring[J][1], q[1], d); d = dot2(ring[J][2], q[2], d); d = dot2(ring[J][3], q[3], d);
    x[I] = d;
    if constexpr ((I & 1) == 1) x[I] = tr<XOR8, 8>(x[I - 1], x[I]);
    if constexpr ((I & 3) == 3) x[I] = tr<XOR7, 4>(x[I - 2], x[I]);
    if constexpr ((I & 7) == 7) x[I] = tr<XOR2, 2>(x[I - 4], x[I]);
    if constexpr (I == 15) x[I] = rbf(rbf(tr<XOR1, 1>(x[I - 8], x[I])) * SCALE);
    asm volatile("" : "+v"(x[I]));
    if constexpr (J == 15) sc[14] = x[15];
    if constexpr (J == 31) sc[15] = x[15];
  }
  template <int J> __device__ __forceinline__ void consume_v() {
    const float pb = dpp<BCAST + bitrev4(J & 15)>(sc[J >> 4]);
    f32x2 t[4];
#pragma unroll
    for (int k = 0; k < 4; ++k) t[k] = __builtin_elementwise_fma(f32x2{pb, pb}, f32x2{bf_lo(ring[J][k]), bf_hi(ring[J][k])}, f32x2{acc[2 * k], acc[2 * k + 1]});
    asm volatile("" : "+v"(t[0]), "+v"(t[1]), "+v"(t[2]), "+v"(t[3]));
#pragma unroll
    for (int k = 0; k < 4; ++k) { acc[2 * k] = t[k][0]; acc[2 * k + 1] = t[k][1]; }
  }
  __device__ __forceinline__ void rotate() {
#pragma unroll
    for (int k = 0; k < 14; ++k) sc[k] = sc[k + 2];
  }
  __device__ __forceinline__ void softmax() {
    float mx = sc[0];
#pragma unroll
    for (int k = 1; k < 16; ++k) mx = fmaxf(mx, sc[k]);
    m = xrow_max(row16_max(mx));
    l = 0.f;
#pragma unroll
    for (int k = 0; k < 16; ++k) { sc[k] = __builtin_amdgcn_exp2f((sc[k] - m) * LOG2E); l += sc[k]; }
  }
  template <int J> __device__ __forceinline__ void fill() {
    if constexpr (J < 32) {
      if constexpr (J >= R) asm volatile("s_waitcnt vmcnt(%0)" :: "n"(R - 1) : "memory");
      if constexpr (J == R || J == 8 || J == 16 || J == 24 || J == 31) PST(J == R ? 1 : J == 31 ? 5 : J / 8 + 1);
      issue<J, 0>(0);
      SB();
      fill<J + 1>();
    }
  }
  template <int J> __device__ __forceinline__ void first() {
    if constexpr (J < 32) {
      consume_k<J>();
      if constexpr (J & 1) {
        asm volatile("s_waitcnt vmcnt(%0)" :: "n"(R - 1) : "memory");
        issue<32 + J / 2, 0>(0);
      }
      SB();
      first<J + 1>();
    }
  }
  static constexpr int NI2 = 16 + R;
  static constexpr bool issues(int J) { return (J + 1) * NI2 / 32 > J * NI2 / 32; }
  static constexpr int pace2(int J) {
    int kv = 0, n = 0;
    for (int t = J - 1; t >= 0; --t) {
      if (t < 24) ++n;
      if (issues(t)) { if (++kv == R) return n; ++n; }
    }
    return n + (R - 1 - kv);
  }
  template <int J> __device__ __forceinline__ void second() {
    if constexpr (J < 32) {
      if constexpr (issues(J)) {
        asm volatile("s_waitcnt vmcnt(%0)" :: "n"(pace2(J)) : "memory");
        issue<16 + J * NI2 / 32, 0>(32);
        SB();
      }
      if constexpr (J < 24) draw[J / 8][J % 8] = __builtin_amdgcn_raw_buffer_load_b128(rd, J < 16 ? doff : doff2, (J % 8) * (D * 2) + (J / 8) * 256, W_AUX);
      SB();
      consume_k<J>();
      SB();
      second<J + 1>();
    }
  }
  template <int J, int PH> __device__ __forceinline__ void steady(int n0) {
    if constexpr (J < 32) {
      asm volatile("s_waitcnt vmcnt(%0)" :: "n"(R - 1) : "memory");
      issue<J + R, PH>(n0);
      SB();
      if constexpr (PH < 2) consume_k<J>(); else consume_v<J>();
      SB();
      steady<J + 1, PH>(n0);
    }
  }
};

struct Args {
  const unsigned short *x, *kc, *vc, *wq, *wo, *wg, *wu, *wd;
  unsigned short* out;
  unsigned char* ws;
};

__device__ __forceinline__ void gu_frags(LAS unsigned char* L, int tile, int lane, u32x4 (&gf)[20]) {
  const int g = lane >> 4, q = (lane >> 2) & 3, p = lane & 3;
  LAS unsigned char* a = L + (p < 2 ? L_G : L_U) + (8 * g + q) * 112 + tile * 16 + (p & 1) * 8;
#pragma unroll
  for (int s = 0; s < 20; ++s) {
    const s16x4 t0 = __builtin_amdgcn_ds_read_tr16_b64_v4i16((LAS s16x4*)(a + s * 3584));
    const s16x4 t1 = __builtin_amdgcn_ds_read_tr16_b64_v4i16((LAS s16x4*)(a + s * 3584 + 448));
    const u32x2 u0 = __builtin_bit_cast(u32x2, t0), u1 = __builtin_bit_cast(u32x2, t1);
    gf[s] = u32x4{u0[0], u0[1], u1[0], u1[1]};
  }
}
__device__ __forceinline__ void gu_tile(LAS unsigned char* L, int tile, int lane, const u32x4 (&gf)[20]) {
  f32x4 acc = {0.f, 0.f, 0.f, 0.f};
  const LAS u32x4* hn = (const LAS u32x4*)(L + L_HN) + (lane >> 4);
#pragma unroll
  for (int s = 0; s < 20; ++s) acc = mfma(hn[4 * s], gf[s], acc);
  const float g = rbf(acc[0]), u = rbf(dpp<XOR8>(acc[0]));
  const float z = 0.7978845608028654f * (g + 0.044715f * g * g * g);
  const float e = __builtin_amdgcn_exp2f(z * (2.f * LOG2E));
  const float th = 1.f - 2.f * __builtin_amdgcn_rcpf(e + 1.f);
  const float ge = rbf(0.5f * g * (1.f + th));
  if (lane < 8) ((LAS unsigned short*)(L + L_ACT))[tile * 8 + lane] = f2bf(ge * u);
}
template <int NB> __device__ __forceinline__ void gemv_blocks(const u32x4 af, const u32x4* fr, LAS unsigned char* dst, int lane) {
  u32x4 am[8];
#pragma unroll
  for (int e = 0; e < 8; ++e)
#pragma unroll
    for (int k = 0; k < 4; ++k) am[e][k] = (lane & 7) == e ? af[k] : 0u;
  LAS f32x4* o = (LAS f32x4*)(dst + (lane & 15) * 32 + ((lane >> 4) & 1) * 16);
#pragma unroll
  for (int n = 0; n < NB; ++n) {
    f32x4 d = {0.f, 0.f, 0.f, 0.f};
#pragma unroll
    for (int e = 0; e < 8; ++e) d = mfma(am[e], fr[n * 8 + e], d);
    o[n * 32] = d;
  }
}
template <int NP> __device__ __forceinline__ void publish_cols(LAS unsigned char* L, rsrc_t rw, int base, int b, int h, int t, unsigned tag) {
  const int j = t >> 3, u = t & 7;
  const LAS float* p = (const LAS float*)(L + L_RB) + 20 * j + 3 * u;
  float s0 = p[0], s1 = p[1], s2 = p[2];
#pragma unroll
  for (int k = 1; k < NP; ++k) { s0 += p[k * 640]; s1 += p[k * 640 + 1]; s2 += p[k * 640 + 2]; }
  const u32x4 v = {u < 7 ? f2u(s0) : 0u, u < 7 ? f2u(s1) : 0u, u < 6 ? f2u(s2) : 0u, tag};
  __builtin_amdgcn_raw_buffer_store_b128(v, rw, base + ((b * 32 + j) * 32 + h) * 128 + u * 16, 0, SC1);
}
template <bool FAR> __device__ __forceinline__ void gather_cols(rsrc_t rw, int base, int b, int j, int lane, unsigned tag, float (&s)[3]) {
  u32x4 v[4];
  sweep<4, FAR>(rw, base + (b * 32 + j) * 4096 + lane * 16, tag, v);
#pragma unroll
  for (int e = 0; e < 3; ++e) {
    float x = (u2f(v[0][e]) + u2f(v[1][e])) + (u2f(v[2][e]) + u2f(v[3][e]));
    x += dpp<XOR8>(x);
    s[e] = xrow_sum(x);
  }
}

__global__ void __launch_bounds__(512, 2) k_block(Args a) {
  __shared__ __attribute__((aligned(16))) unsigned char lds_[L_END];
  LAS unsigned char* L = (LAS unsigned char*)lds_;
  unsigned long long st[8] = {0, 0, 0, 0, 0, 0, 0, 0};
  STAMP(0); PST(0); VST(0);
  const int bid = blockIdx.x, h = bid & 31, b = bid >> 5;
  const int tid = threadIdx.x, lane = tid & 63, w = __builtin_amdgcn_readfirstlane(tid >> 6), r = lane >> 4, c = lane & 15;
  const int ch0 = 27 * (h & 7) + 7 * (h >> 3), nch = (h >> 3) == 3 ? 6 : 7;
  const rsrc_t rw = __builtin_amdgcn_make_buffer_rsrc((void*)a.ws, 0, WS_END, RS3);

  if (w < 4) {
    __builtin_amdgcn_s_setprio(2);
    Stream s;
    s.rk = __builtin_amdgcn_make_buffer_rsrc((void*)(a.kc + (size_t)bid * SK * DH), 0, SK * DH * 2, RS3);
    s.rv = __builtin_amdgcn_make_buffer_rsrc((void*)(a.vc + (size_t)bid * SK * DH), 0, SK * DH * 2, RS3);
    s.rd = __builtin_amdgcn_make_buffer_rsrc((void*)(a.wd + (size_t)ch0 * 8 * D), 0, NO_W ? 0 : nch * 8 * D * 2, RS3);
    s.voff = w * 1024 + lane * 16;
    const int kb = w & 1, nb0 = (w >> 1) * 3;
    s.doff = (32 * kb + 8 * r) * (D * 2) + nb0 * 256 + c * 16;
    s.doff2 = w < 2 ? s.doff : OOB - 65536;
    s.c = c;
#pragma unroll
    for (int k = 0; k < 8; ++k) s.acc[k] = 0.f;
#pragma unroll
    for (int k = 0; k < 16; ++k) s.sc[k] = 0.f;
    s.st = st;
    s.fill<0>();
    STAMP(1);
    WG_BARRIER();
    {
      const u32x4 qq = *(const LAS u32x4*)(L + L_Q + c * 16);
#pragma unroll
      for (int k = 0; k < 4; ++k) s.q[k] = qq[k];
    }
    const unsigned tag = __builtin_amdgcn_readfirstlane(*(const LAS unsigned*)(L + L_TAG));
    SB();
    PST(6); VST(1);
    s.first<0>();
    s.rotate();
    s.second<0>();
    u32x4 df[24];
    to_frags(s.draw[0], df); to_frags(s.draw[1], df + 8); to_frags(s.draw[2], df + 16);
#pragma unroll
    for (int k = 0; k < 24; ++k) asm volatile("" : "+v"(df[k]));
    SB();
    for (int n0 = 64; n0 < 224; n0 += 32) { if (n0 == 128) VST(2); s.rotate(); s.steady<0, 0>(n0); }
    s.rotate();
    s.steady<0, 1>(224);
    VST(3);
    s.softmax();
    SB();
    for (int n0 = 256; n0 < 512; n0 += 32) { if (n0 == 320) VST(4); if (n0 == 384) VST(5); if (n0 == 448) VST(6); s.steady<0, 2>(n0); s.rotate(); }
    VST(7);
    {
      const float a0 = swap16_add(s.acc[0], s.acc[1]), a1 = swap16_add(s.acc[2], s.acc[3]);
      const float a2 = swap16_add(s.acc[4], s.acc[5]), a3 = swap16_add(s.acc[6], s.acc[7]);
      const float b0 = swap32_add(a0, a1), b1 = swap32_add(a2, a3);
      const float Ls = wave_sum(s.l);
      LAS float* pa = (LAS float*)(L + L_PA) + w * 128 + 8 * c + r;
      pa[0] = b0; pa[4] = b1;
      if (lane == 0) { ((LAS float*)(L + L_PML))[w] = s.m; ((LAS float*)(L + L_PML))[4 + w] = Ls; }
    }
    STAMP(2);
    WG_BARRIER();
    u32x4 gf[20];
    float hres[3] = {0.f, 0.f, 0.f};
    const int u = lane & 7;
    if (w != 0) gu_frags(L, 3 + w, lane, gf);
    else {
#pragma unroll
      for (int k = 0; k < 20; ++k) gf[k] = u32x4{0u, 0u, 0u, 0u};
      const rsrc_t rx = __builtin_amdgcn_make_buffer_rsrc((void*)(a.x + b * D + 20 * h), 0, 40, RS3);
#pragma unroll
      for (int e = 0; e < 3; ++e) hres[e] = bf2f(__builtin_amdgcn_raw_buffer_load_b16(rx, (3 * u + e) * 2, 0, 0));
    }
    WG_BARRIER();
    STAMP(3); TST(0);
    if (w == 0) {
      float sa[3];
      if (DELAY_A) __builtin_amdgcn_s_sleep(DELAY_A);
      gather_cols<true>(rw, WS_PA, b, h, lane, tag, sa);
      STAMP(4); TST(1);
#pragma unroll
      for (int e = 0; e < 3; ++e) hres[e] = rbf(hres[e] + rbf(sa[e]));
      __builtin_amdgcn_raw_buffer_store_b128(u32x4{f2u(hres[0]), f2u(hres[1]), f2u(hres[2]), tag + 1u}, rw,
                                             lane < 8 ? WS_HB + b * HB_STRIDE + h * 128 + lane * 16 : OOB, 0, SC1);
      if (DELAY_B) __builtin_amdgcn_s_sleep(DELAY_B);
      TST(2);
      u32x4 v[4];
      sweep<4, false>(rw, WS_HB + b * HB_STRIDE + lane * 16, tag + 1u, v);
      STAMP(5); TST(3);
      float sum = 0.f;
#pragma unroll
      for (int i = 0; i < 4; ++i) sum += (u2f(v[i][0]) + u2f(v[i][1])) + u2f(v[i][2]);
      const float mean = wave_sum(sum) * (1.f / D);
      float v01 = 0.f, v2 = 0.f;
#pragma unroll
      for (int i = 0; i < 4; ++i) {
        const float d0 = u2f(v[i][0]) - mean, d1 = u2f(v[i][1]) - mean, d2 = u2f(v[i][2]) - mean;
        v01 = fmaf(d0, d0, v01); v01 = fmaf(d1, d1, v01); v2 = fmaf(d2, d2, v2);
      }
      const float var = wave_sum((u < 7 ? v01 : 0.f) + (u < 6 ? v2 : 0.f)) * (1.f / D);
      const float mb = rbf(mean), ri = __builtin_amdgcn_rcpf(rbf(__builtin_amdgcn_sqrtf(rbf(rbf(var) + 1e-5f))));
      LAS unsigned short* hn = (LAS unsigned short*)(L + L_HN) + 20 * (lane >> 3) + 3 * u;
      LAS unsigned short* dump = (LAS unsigned short*)(L + L_PA);
      LAS unsigned short* h01 = u < 7 ? hn : dump;
      LAS unsigned short* h2 = u < 6 ? hn : dump;
#pragma unroll
      for (int i = 0; i < 4; ++i) {
        h01[160 * i] = f2bf(rbf(u2f(v[i][0]) - mb) * ri);
        h01[160 * i + 1] = f2bf(rbf(u2f(v[i][1]) - mb) * ri);
        h2[160 * i + 2] = f2bf(rbf(u2f(v[i][2]) - mb) * ri);
      }
      TST(4);
    }
    WG_BARRIER();
    if (w != 0) gu_tile(L, 3 + w, lane, gf);
    WG_BARRIER();
    TST(5);
    {
      const u32x4 af = *(const LAS u32x4*)(L + L_ACT + kb * 64 + r * 16);
      if (w < 2) gemv_blocks<3>(af, df, L + L_RB + kb * 2560, lane);
      else gemv_blocks<2>(af, df, L + L_RB + kb * 2560 + 3 * 512, lane);
    }
    TST(6);
    WG_BARRIER();
    STAMP(6);
    if (w == 0) {
      float sc[3];
      if (DELAY_C) __builtin_amdgcn_s_sleep(DELAY_C);
      gather_cols<false>(rw, WS_PC, b, h, lane, tag + 1u, sc);
      TST(7);
      const rsrc_t ry = __builtin_amdgcn_make_buffer_rsrc((void*)(a.out + b * D + 20 * h), 0, 40, RS3);
#pragma unroll
      for (int e = 0; e < 3; ++e) __builtin_amdgcn_raw_buffer_store_b16(f2bf(hres[e] + rbf(sc[e])), ry, lane < 8 ? (3 * u + e) * 2 : OOB, 0, 0);
      if (bid == 0 && lane == 0) __hip_atomic_store((GAS unsigned*)(a.ws + WS_SEED), tag + 1u, __ATOMIC_RELAXED, __HIP_MEMORY_SCOPE_AGENT);
    }
  } else {
    const int hw = w - 4;
    if (hw == 0) {
      const int hq = bid >> 3, ks = bid & 7;
      const unsigned seed = __builtin_amdgcn_raw_buffer_load_b32(rw, WS_SEED, 0, SC1);
      const rsrc_t rx = __builtin_amdgcn_make_buffer_rsrc((void*)a.x, 0, B * D * 2, RS3);
      const rsrc_t rq = __builtin_amdgcn_make_buffer_rsrc((void*)(a.wq + hq * DH), 0, (80 * (ks + 1) - 1) * (H * DH * 2) + DH * 2, RS3);
      u32x4 xs[10], xa[3], raw[3][8];
      const int xrow = (c & 7) * (D * 2);
#pragma unroll
      for (int i = 0; i < 10; ++i) xs[i] = __builtin_amdgcn_raw_buffer_load_b128(rx, (lane >> 3) * (D * 2) + (lane & 7) * 16 + i * 128, 0, 0);
#pragma unroll
      for (int k = 0; k < 3; ++k) xa[k] = __builtin_amdgcn_raw_buffer_load_b128(rx, (32 * k + 8 * r < 80) ? xrow + (80 * ks + 32 * k + 8 * r) * 2 : OOB, 0, 0);
      SB();
#pragma unroll
      for (int k = 0; k < 3; ++k)
#pragma unroll
        for (int i = 0; i < 8; ++i)
          raw[k][i] = __builtin_amdgcn_raw_buffer_load_b128(rq, 8 * r * (H * DH * 2) + c * 16, (80 * ks + 32 * k + i) * (H * DH * 2), W_AUX);
      SB();
      PST(1);
      float sum = 0.f, ssq = 0.f;
#pragma unroll
      for (int i = 0; i < 10; ++i)
#pragma unroll
        for (int k = 0; k < 4; ++k) { sum = dot2(xs[i][k], 0x3f803f80u, sum); ssq = dot2(xs[i][k], xs[i][k], ssq); }
      sum += dpp<XOR1>(sum); ssq += dpp<XOR1>(ssq); sum += dpp<XOR2>(sum); ssq += dpp<XOR2>(ssq); sum += dpp<XOR7>(sum); ssq += dpp<XOR7>(ssq);
      const float mean = sum * (1.f / D);
      const float var = fmaxf(fmaf(-mean, mean, ssq * (1.f / D)), 0.f);
      PST(2);
      const float mb = u2f(__builtin_amdgcn_ds_bpermute((c & 7) * 32, f2u(rbf(mean))));
      const float ri = u2f(__builtin_amdgcn_ds_bpermute((c & 7) * 32, f2u(__builtin_amdgcn_rcpf(rbf(__builtin_amdgcn_sqrtf(rbf(rbf(var) + 1e-5f)))))));
      PST(3);
      f32x4 d[8];
#pragma unroll
      for (int e = 0; e < 8; ++e) d[e] = f32x4{0.f, 0.f, 0.f, 0.f};
#pragma unroll
      for (int k = 0; k < 3; ++k) {
        u32x4 af;
#pragma unroll
        for (int i = 0; i < 4; ++i) af[i] = (32 * k + 8 * r < 80) ? pk2(rbf(bf_lo(xa[k][i]) - mb) * ri, rbf(bf_hi(xa[k][i]) - mb) * ri) : 0u;
        u32x4 fr[8];
        to_frags(raw[k], fr);
#pragma unroll
        for (int e = 0; e < 8; ++e) d[e] = mfma(af, fr[e], d[e]);
      }
      PST(4);
      const unsigned tag = __builtin_amdgcn_readfirstlane(seed) + 1u;
      const bool up = r >= 2;
#pragma unroll
      for (int i = 0; i < 4; ++i) {
        const int off = WS_QP + ((hq * 8 + ((4 * r + i) & 7)) * 8 + ks) * 1024 + (c + (up ? 16 : 0)) * 16;
        const float e0 = up ? d[4][i] : d[0][i], e1 = up ? d[5][i] : d[1][i], e2 = up ? d[6][i] : d[2][i], e3 = up ? d[7][i] : d[3][i];
        __builtin_amdgcn_raw_buffer_store_b128(u32x4{f2u(e0), tag, f2u(e1), tag}, rw, off, 0, SC1);
        __builtin_amdgcn_raw_buffer_store_b128(u32x4{f2u(e2), tag, f2u(e3), tag}, rw, off + 512, 0, SC1);
      }
      STAMP(4); PST(5);
      for (int spin = 0; spin < (1 << 20); ++spin) {
        __builtin_amdgcn_s_sleep(QPOLL_SLEEP);
        const u32x4 t = __builtin_amdgcn_raw_buffer_load_b128(rw, WS_QP + (h * 8 + b) * 8192 + (lane >> 3) * 1024 + 896 + (lane & 7) * 16, 0, SC1);
        if (__builtin_amdgcn_ballot_w64(t[3] != tag) == 0ull) break;
        CBAR();
      }
      u32x4 v[8];
      sweep<8, false, true>(rw, WS_QP + (h * 8 + b) * 8192 + lane * 16, tag, v);
      PST(6);
      float q0 = 0.f, q1 = 0.f;
#pragma unroll
      for (int i = 0; i < 8; ++i) { q0 += u2f(v[i][0]); q1 += u2f(v[i][2]); }
      ((LAS unsigned*)(L + L_Q))[4 * c + 2 * (r & 1) + (r >> 1)] = pk2(q0, q1);
      if (lane == 0) *(LAS unsigned*)(L + L_TAG) = tag;
      if (lane < 4) ((LAS unsigned*)(L + L_ACT))[28 + lane] = 0u;
    }
    STAMP(1);
    WG_BARRIER();
    const unsigned tag = __builtin_amdgcn_readfirstlane(*(const LAS unsigned*)(L + L_TAG));
    u32x4 of[40];
    {
      const rsrc_t ro = __builtin_amdgcn_make_buffer_rsrc((void*)(a.wo + (size_t)h * DH * D), 0, NO_W ? 0 : DH * D * 2, RS3);
      const rsrc_t rg = __builtin_amdgcn_make_buffer_rsrc((void*)(hw < 2 ? a.wg : a.wu), 0, NO_W ? 0 : D * F * 2, RS3);
      const int ooff = (32 * hw + 8 * r) * (D * 2) + c * 16;
      int id = (hw & 1) * 35 * 64 + lane;
      int row = id / 7, pc = id - 7 * row;
      LAS unsigned char* dst = L + (hw < 2 ? L_G : L_U) + (hw & 1) * 35 * 1024;
#pragma unroll
      for (int n = 0; n < 5; ++n) {
        u32x4 raw[8];
#pragma unroll
        for (int i = 0; i < 8; ++i) {
          raw[i] = __builtin_amdgcn_raw_buffer_load_b128(ro, ooff + n * 256, i * (D * 2), W_AUX);
          CBAR(); __builtin_amdgcn_s_sleep(HSLEEP); CBAR();
        }
        to_frags(raw, of + 8 * n);
#pragma unroll
        for (int e = 0; e < 8; ++e) asm volatile("" : "+v"(of[8 * n + e]));
        for (int i = 0; i < 7; ++i) {
          __builtin_amdgcn_raw_ptr_buffer_load_lds(rg, (LAS void*)dst, 16, pc < nch ? row * (F * 2) + (ch0 + pc) * 16 : OOB, 0, 0, W_AUX);
          dst += 1024;
          row += 9; pc += 1;
          if (pc >= 7) { pc -= 7; row += 1; }
          CBAR(); __builtin_amdgcn_s_sleep(HSLEEP); CBAR();
        }
      }
    }
    asm volatile("s_waitcnt vmcnt(0)" ::: "memory");
    STAMP(2);
    WG_BARRIER();
    TST(0);
    {
      const f32x4 mm = *(const LAS f32x4*)(L + L_PML), ll = *(const LAS f32x4*)(L + L_PML + 16);
      f32x4 pv[4][2];
#pragma unroll
      for (int ww = 0; ww < 4; ++ww) {
        const LAS f32x4* p = (const LAS f32x4*)(L + L_PA + (ww * 128 + 32 * hw + 8 * r) * 4);
        pv[ww][0] = p[0]; pv[ww][1] = p[1];
      }
      const float M = fmaxf(fmaxf(mm[0], mm[1]), fmaxf(mm[2], mm[3]));
      float wt[4], Ls = 0.f;
#pragma unroll
      for (int ww = 0; ww < 4; ++ww) { wt[ww] = __builtin_amdgcn_exp2f((mm[ww] - M) * LOG2E); Ls = fmaf(wt[ww], ll[ww], Ls); }
      const float inv = __builtin_amdgcn_rcpf(Ls);
      f32x4 o0 = pv[0][0] * wt[0], o1 = pv[0][1] * wt[0];
#pragma unroll
      for (int ww = 1; ww < 4; ++ww) { o0 += pv[ww][0] * wt[ww]; o1 += pv[ww][1] * wt[ww]; }
      o0 *= inv; o1 *= inv;
      const u32x4 af = {pk2(o0[0], o0[1]), pk2(o0[2], o0[3]), pk2(o1[0], o1[1]), pk2(o1[2], o1[3])};
      TST(1);
      gemv_blocks<5>(af, of, L + L_RB + hw * 2560, lane);
      TST(2);
    }
    WG_BARRIER();
    TST(3);
    publish_cols<4>(L, rw, WS_PA, b, h, tid - 256, tag);
    STAMP(3); TST(4);
    u32x4 gf[20];
    gu_frags(L, hw, lane, gf);
    WG_BARRIER();
    TST(5);
    gu_tile(L, hw, lane, gf);
    STAMP(5); TST(6);
    WG_BARRIER();
    WG_BARRIER();
    publish_cols<2>(L, rw, WS_PC, b, h, tid - 256, tag + 1u);
    STAMP(6); TST(7);
  }
  STAMP(7); PST(7);
  if (bid == 5 && w == 0) { const unsigned long long until = st[0] + 20000; while (__builtin_amdgcn_s_memrealtime() < until) __builtin_amdgcn_s_sleep(8); }
}
}

extern "C" void kernel_launch(void* const* d_in, const int* in_sizes, int n_in, void* d_out, int out_size, void* d_ws,
                              size_t ws_size, hipStream_t stream) {
  Args a;
  a.x = (const unsigned short*)d_in[0];
  a.kc = (const unsigned short*)d_in[1];
  a.vc = (const unsigned short*)d_in[2];
  a.wq = (const unsigned short*)d_in[3];
  a.wo = (const unsigned short*)d_in[4];
  a.wg = (const unsigned short*)d_in[6];
  a.wu = (const unsigned short*)d_in[7];
  a.wd = (const unsigned short*)d_in[8];
  a.out = (unsigned short*)d_out;
  a.ws = (unsigned char*)d_ws;
  k_block<<<B * H, 512, 0, stream>>>(a);
}
```

```cpp
#define STAMPS 1
#include <hip/hip_runtime.h>
#include <hip/hip_bf16.h>
#include <stdint.h>

#define RDEPTH 5
#define W_AUX 0
#define QPOLL_SLEEP 4
#define NO_W 0
#define PKV 0
#define DELAY_A 18
#define DELAY_B 12
#define DELAY_C 20
#define HSLEEP 26
#define POLL_SLEEP 0
#define POLL_SLEEP_FAR 12
#define POLL_FAR 16

namespace {
constexpr int B = 8, D = 640, H = 32, SK = 4096, DH = 128, F = 1728;
typedef unsigned u32x4 __attribute__((ext_vector_type(4)));
typedef unsigned u32x2 __attribute__((ext_vector_type(2)));
typedef float f32x4 __attribute__((ext_vector_type(4)));
typedef float f32x2 __attribute__((ext_vector_type(2)));
typedef short s16x4 __attribute__((ext_vector_type(4)));
typedef __bf16 bf16x2 __attribute__((ext_vector_type(2)));
typedef __bf16 bf16x8 __attribute__((ext_vector_type(8)));
#define LAS __attribute__((address_space(3)))
#define GAS __attribute__((address_space(1)))
typedef __amdgpu_buffer_rsrc_t rsrc_t;

__device__ __forceinline__ float bf_lo(unsigned u) { return __builtin_bit_cast(float, u << 16); }
__device__ __forceinline__ float bf_hi(unsigned u) { return __builtin_bit_cast(float, u & 0xffff0000u); }
__device__ __forceinline__ float bf2f(unsigned short u) { return __builtin_bit_cast(float, (unsigned)u << 16); }
__device__ __forceinline__ unsigned short f2bf(float f) { return __builtin_bit_cast(unsigned short, (__bf16)f); }
__device__ __forceinline__ float rbf(float f) { return bf2f(f2bf(f)); }
__device__ __forceinline__ unsigned pk2(float lo, float hi) {
  bf16x2 v = {(__bf16)lo, (__bf16)hi};
  return __builtin_bit_cast(unsigned, v);
}
__device__ __forceinline__ float u2f(unsigned u) { return __builtin_bit_cast(float, u); }
__device__ __forceinline__ unsigned f2u(float f) { return __builtin_bit_cast(unsigned, f); }
__device__ __forceinline__ float dot2(unsigned k, unsigned q, float acc) {
  return __builtin_amdgcn_fdot2_f32_bf16(__builtin_bit_cast(bf16x2, k), __builtin_bit_cast(bf16x2, q), acc, false);
}
__device__ __forceinline__ f32x4 mfma(u32x4 a, u32x4 b, f32x4 c) {
  return __builtin_amdgcn_mfma_f32_16x16x32_bf16(__builtin_bit_cast(bf16x8, a), __builtin_bit_cast(bf16x8, b), c, 0, 0, 0);
}
template <int CTRL> __device__ __forceinline__ float dpp(float x) {
  return __builtin_bit_cast(float, __builtin_amdgcn_mov_dpp(__builtin_bit_cast(int, x), CTRL, 0xf, 0xf, true));
}
template <int CTRL> __device__ __forceinline__ unsigned dppu(unsigned x) { return (unsigned)__builtin_amdgcn_mov_dpp((int)x, CTRL, 0xf, 0xf, true); }
constexpr int XOR1 = 0xB1, XOR2 = 0x4E, XOR7 = 0x141, XOR8 = 0x128, BCAST = 0x150;
__device__ __forceinline__ float row16_sum(float x) { x += dpp<XOR1>(x); x += dpp<XOR2>(x); x += dpp<XOR7>(x); x += dpp<XOR8>(x); return x; }
__device__ __forceinline__ float row16_max(float x) {
  x = fmaxf(x, dpp<XOR1>(x)); x = fmaxf(x, dpp<XOR2>(x)); x = fmaxf(x, dpp<XOR7>(x)); x = fmaxf(x, dpp<XOR8>(x)); return x;
}
__device__ __forceinline__ float swap16_add(float a, float b) {
  auto s = __builtin_amdgcn_permlane16_swap(f2u(a), f2u(b), false, false);
  return u2f(s[0]) + u2f(s[1]);
}
__device__ __forceinline__ float swap32_add(float a, float b) {
  auto s = __builtin_amdgcn_permlane32_swap(f2u(a), f2u(b), false, false);
  return u2f(s[0]) + u2f(s[1]);
}
__device__ __forceinline__ float xrow_sum(float x) { x = swap16_add(x, x); return swap32_add(x, x); }
__device__ __forceinline__ float xrow_max(float x) {
  auto s = __builtin_amdgcn_permlane16_swap(f2u(x), f2u(x), false, false);
  x = fmaxf(u2f(s[0]), u2f(s[1]));
  auto t = __builtin_amdgcn_permlane32_swap(f2u(x), f2u(x), false, false);
  return fmaxf(u2f(t[0]), u2f(t[1]));
}
__device__ __forceinline__ float wave_sum(float x) { return xrow_sum(row16_sum(x)); }
constexpr int bitrev4(int j) { return ((j & 1) << 3) | ((j & 2) << 1) | ((j & 4) >> 1) | ((j & 8) >> 3); }

#define SB() __builtin_amdgcn_sched_barrier(0)
#define CBAR() asm volatile("" ::: "memory")
#define WG_BARRIER() do { asm volatile("s_waitcnt lgkmcnt(0)" ::: "memory"); __builtin_amdgcn_s_barrier(); CBAR(); } while (0)
#define PSTAMP 0
#define VSTAMP 0
#define VST(i)
#define TSTAMP 0
#define TST(i)
#define STAMP(i) st[i] = __builtin_amdgcn_s_memrealtime()
#define PST(i)

constexpr int R = RDEPTH;
constexpr int NT = 2, SC1 = 16;
constexpr int RS3 = 0x00020000;
constexpr int OOB = 0x7ffffff0;
constexpr float SCALE = 0.08838834764831845f, LOG2E = 1.4426950408889634f;

constexpr int WS_SEED = 0;
constexpr int WS_QP = 4096;
constexpr int WS_PA = WS_QP + 32 * 8 * 8 * 1024;
constexpr int WS_HB = WS_QP, HB_STRIDE = 8192;
constexpr int WS_PC = WS_PA;
constexpr int WS_END = WS_PA + 8 * 32 * 32 * 128;
constexpr int WS_STAMPS = 16 << 20;
constexpr int L_G = 0, L_U = 640 * 112, L_RB = 2 * 640 * 112;
constexpr int L_PA = L_RB + 4 * 640 * 4;
constexpr int L_PML = L_PA + 4 * 128 * 4;
constexpr int L_Q = L_PML + 32;
constexpr int L_TAG = L_Q + 256;
constexpr int L_HN = L_TAG + 16;
constexpr int L_ACT = L_HN + 1280;
constexpr int L_END = L_ACT + 128;
static_assert(L_END <= 163840, "LDS");

__device__ __forceinline__ void to_frags(const u32x4 (&raw)[8], u32x4* fr) {
#pragma unroll
  for (int d = 0; d < 4; ++d) {
    u32x4 lo, hi;
#pragma unroll
    for (int kp = 0; kp < 4; ++kp) {
      lo[kp] = __builtin_amdgcn_perm(raw[2 * kp + 1][d], raw[2 * kp][d], 0x05040100u);
      hi[kp] = __builtin_amdgcn_perm(raw[2 * kp + 1][d], raw[2 * kp][d], 0x07060302u);
    }
    fr[2 * d] = lo; fr[2 * d + 1] = hi;
  }
}

template <int N, bool FAR, bool TWO = false> __device__ __forceinline__ void sweep(rsrc_t rs, int voff, unsigned tag, u32x4 (&v)[N]) {
  for (int spin = 0; spin < (1 << 20); ++spin) {
#pragma unroll
    for (int i = 0; i < N; ++i) v[i] = __builtin_amdgcn_raw_buffer_load_b128(rs, voff + (i & 3) * 1024, (i >> 2) * 4096, SC1);
    unsigned bad = 0u;
#pragma unroll
    for (int i = 0; i < N; ++i) { bad |= v[i][3] ^ tag; if (TWO) bad |= v[i][1] ^ tag; }
    const unsigned long long m = __builtin_amdgcn_ballot_w64(bad != 0u);
    if (m == 0ull) break;
    if (FAR && __builtin_popcountll(m) > POLL_FAR) __builtin_amdgcn_s_sleep(POLL_SLEEP_FAR);
    else if (POLL_SLEEP) __builtin_amdgcn_s_sleep(POLL_SLEEP);
    CBAR();
  }
}

struct Stream {
  rsrc_t rk, rv, rd;
  int voff, doff, doff2;
  u32x4 ring[32];
  u32x4 draw[3][8];
  unsigned q[4];
  float x[16];
  float sc[16];
  float m, l, acc[8];
  int c;
  unsigned long long* st;

  template <int J, int PH> __device__ __forceinline__ void issue(int n0) {
    if constexpr (PH == 0 || (PH == 1 && J < 32)) ring[J & 31] = __builtin_amdgcn_raw_buffer_load_b128(rk, voff, (n0 + J) * 4096, NT);
    else ring[J & 31] = __builtin_amdgcn_raw_buffer_load_b128(rv, voff, (n0 + J - 256) * 4096, NT);
  }
  template <int CTRL, int BIT> __device__ __forceinline__ float tr(float lo, float hi) {
    const float t1 = lo + dpp<CTRL>(lo), t2 = hi + dpp<CTRL>(hi);
    return (c & BIT) ? t2 : t1;
  }
  template <int J> __device__ __forceinline__ void consume_k() {
    constexpr int I = J & 15;
    float d = dot2(ring[J][0], q[0], 0.f);
    d = dot2(ring[J][1], q[1], d); d = dot2(ring[J][2], q[2], d); d = dot2(ring[J][3], q[3], d);
    x[I] = d;
    if constexpr ((I & 1) == 1) x[I] = tr<XOR8, 8>(x[I - 1], x[I]);
    if constexpr ((I & 3) == 3) x[I] = tr<XOR7, 4>(x[I - 2], x[I]);
    if constexpr ((I & 7) == 7) x[I] = tr<XOR2, 2>(x[I - 4], x[I]);
    if constexpr (I == 15) x[I] = rbf(rbf(tr<XOR1, 1>(x[I - 8], x[I])) * SCALE);
    asm volatile("" : "+v"(x[I]));
    if constexpr (J == 15) sc[14] = x[15];
    if constexpr (J == 31) sc[15] = x[15];
  }
  template <int J> __device__ __forceinline__ void consume_v() {
    const float pb = dpp<BCAST + bitrev4(J & 15)>(sc[J >> 4]);
    f32x2 t[4];
#pragma unroll
    for (int k = 0; k < 4; ++k) t[k] = __builtin_elementwise_fma(f32x2{pb, pb}, f32x2{bf_lo(ring[J][k]), bf_hi(ring[J][k])}, f32x2{acc[2 * k], acc[2 * k + 1]});
    asm volatile("" : "+v"(t[0]), "+v"(t[1]), "+v"(t[2]), "+v"(t[3]));
#pragma unroll
    for (int k = 0; k < 4; ++k) { acc[2 * k] = t[k][0]; acc[2 * k + 1] = t[k][1]; }
  }
  __device__ __forceinline__ void rotate() {
#pragma unroll
    for (int k = 0; k < 14; ++k) sc[k] = sc[k + 2];
  }
  __device__ __forceinline__ void softmax() {
    float mx = sc[0];
#pragma unroll
    for (int k = 1; k < 16; ++k) mx = fmaxf(mx, sc[k]);
    m = xrow_max(row16_max(mx));
    l = 0.f;
#pragma unroll
    for (int k = 0; k < 16; ++k) { sc[k] = __builtin_amdgcn_exp2f((sc[k] - m) * LOG2E); l += sc[k]; }
  }
  template <int J> __device__ __forceinline__ void fill() {
    if constexpr (J < 32) {
      if constexpr (J >= R) asm volatile("s_waitcnt vmcnt(%0)" :: "n"(R - 1) : "memory");
      if constexpr (J == R || J == 8 || J == 16 || J == 24 || J == 31) PST(J == R ? 1 : J == 31 ? 5 : J / 8 + 1);
      issue<J, 0>(0);
      SB();
      fill<J + 1>();
    }
  }
  template <int J> __device__ __forceinline__ void first() {
    if constexpr (J < 32) {
      consume_k<J>();
      if constexpr (J & 1) {
        asm volatile("s_waitcnt vmcnt(%0)" :: "n"(R - 1) : "memory");
        issue<32 + J / 2, 0>(0);
      }
      SB();
      first<J + 1>();
    }
  }
  static constexpr int NI2 = 16 + R;
  static constexpr bool issues(int J) { return (J + 1) * NI2 / 32 > J * NI2 / 32; }
  static constexpr int pace2(int J) {
    int kv = 0, n = 0;
    for (int t = J - 1; t >= 0; --t) {
      if (t < 24) ++n;
      if (issues(t)) { if (++kv == R) return n; ++n; }
    }
    return n + (R - 1 - kv);
  }
  template <int J> __device__ __forceinline__ void second() {
    if constexpr (J < 32) {
      if constexpr (issues(J)) {
        asm volatile("s_waitcnt vmcnt(%0)" :: "n"(pace2(J)) : "memory");
        issue<16 + J * NI2 / 32, 0>(32);
        SB();
      }
      if constexpr (J < 24) draw[J / 8][J % 8] = __builtin_amdgcn_raw_buffer_load_b128(rd, J < 16 ? doff : doff2, (J % 8) * (D * 2) + (J / 8) * 256, W_AUX);
      SB();
      consume_k<J>();
      SB();
      second<J + 1>();
    }
  }
  template <int J, int PH> __device__ __forceinline__ void steady(int n0) {
    if constexpr (J < 32) {
      asm volatile("s_waitcnt vmcnt(%0)" :: "n"(R - 1) : "memory");
      issue<J + R, PH>(n0);
      SB();
      if constexpr (PH < 2) consume_k<J>(); else consume_v<J>();
      SB();
      steady<J + 1, PH>(n0);
    }
  }
};

struct Args {
  const unsigned short *x, *kc, *vc, *wq, *wo, *wg, *wu, *wd;
  unsigned short* out;
  unsigned char* ws;
};

__device__ __forceinline__ void gu_frags(LAS unsigned char* L, int tile, int lane, u32x4 (&gf)[20]) {
  const int g = lane >> 4, q = (lane >> 2) & 3, p = lane & 3;
  LAS unsigned char* a = L + (p < 2 ? L_G : L_U) + (8 * g + q) * 112 + tile * 16 + (p & 1) * 8;
#pragma unroll
  for (int s = 0; s < 20; ++s) {
    const s16x4 t0 = __builtin_amdgcn_ds_read_tr16_b64_v4i16((LAS s16x4*)(a + s * 3584));
    const s16x4 t1 = __builtin_amdgcn_ds_read_tr16_b64_v4i16((LAS s16x4*)(a + s * 3584 + 448));
    const u32x2 u0 = __builtin_bit_cast(u32x2, t0), u1 = __builtin_bit_cast(u32x2, t1);
    gf[s] = u32x4{u0[0], u0[1], u1[0], u1[1]};
  }
}
__device__ __forceinline__ void gu_tile(LAS unsigned char* L, int tile, int lane, const u32x4 (&gf)[20]) {
  f32x4 acc = {0.f, 0.f, 0.f, 0.f};
  const LAS u32x4* hn = (const LAS u32x4*)(L + L_HN) + (lane >> 4);
#pragma unroll
  for (int s = 0; s < 20; ++s) acc = mfma(hn[4 * s], gf[s], acc);
  const float g = rbf(acc[0]), u = rbf(dpp<XOR8>(acc[0]));
  const float z = 0.7978845608028654f * (g + 0.044715f * g * g * g);
  const float e = __builtin_amdgcn_exp2f(z * (2.f * LOG2E));
  const float th = 1.f - 2.f * __builtin_amdgcn_rcpf(e + 1.f);
  const float ge = rbf(0.5f * g * (1.f + th));
  if (lane < 8) ((LAS unsigned short*)(L + L_ACT))[tile * 8 + lane] = f2bf(ge * u);
}
template <int NB> __device__ __forceinline__ void gemv_blocks(const u32x4 af, const u32x4* fr, LAS unsigned char* dst, int lane) {
  u32x4 am[8];
#pragma unroll
  for (int e = 0; e < 8; ++e)
#pragma unroll
    for (int k = 0; k < 4; ++k) am[e][k] = (lane & 7) == e ? af[k] : 0u;
  LAS f32x4* o = (LAS f32x4*)(dst + (lane & 15) * 32 + ((lane >> 4) & 1) * 16);
#pragma unroll
  for (int n = 0; n < NB; ++n) {
    f32x4 d = {0.f, 0.f, 0.f, 0.f};
#pragma unroll
    for (int e = 0; e < 8; ++e) d = mfma(am[e], fr[n * 8 + e], d);
    o[n * 32] = d;
  }
}
template <int NP> __device__ __forceinline__ void publish_cols(LAS unsigned char* L, rsrc_t rw, int base, int b, int h, int t, unsigned tag) {
  const int j = t >> 3, u = t & 7;
  const LAS float* p = (const LAS float*)(L + L_RB) + 20 * j + 3 * u;
  float s0 = p[0], s1 = p[1], s2 = p[2];
#pragma unroll
  for (int k = 1; k < NP; ++k) { s0 += p[k * 640]; s1 += p[k * 640 + 1]; s2 += p[k * 640 + 2]; }
  const u32x4 v = {u < 7 ? f2u(s0) : 0u, u < 7 ? f2u(s1) : 0u, u < 6 ? f2u(s2) : 0u, tag};
  __builtin_amdgcn_raw_buffer_store_b128(v, rw, base + ((b * 32 + j) * 32 + h) * 128 + u * 16, 0, SC1);
}
template <bool FAR> __device__ __forceinline__ void gather_cols(rsrc_t rw, int base, int b, int j, int lane, unsigned tag, float (&s)[3]) {
  u32x4 v[4];
  sweep<4, FAR>(rw, base + (b * 32 + j) * 4096 + lane * 16, tag, v);
#pragma unroll
  for (int e = 0; e < 3; ++e) {
    float x = (u2f(v[0][e]) + u2f(v[1][e])) + (u2f(v[2][e]) + u2f(v[3][e]));
    x += dpp<XOR8>(x);
    s[e] = xrow_sum(x);
  }
}

__global__ void __launch_bounds__(512, 2) k_block(Args a) {
  __shared__ __attribute__((aligned(16))) unsigned char lds_[L_END];
  LAS unsigned char* L = (LAS unsigned char*)lds_;
  unsigned long long st[8] = {0, 0, 0, 0, 0, 0, 0, 0};
  STAMP(0); PST(0); VST(0);
  const int bid = blockIdx.x, h = bid & 31, b = bid >> 5;
  const int tid = threadIdx.x, lane = tid & 63, w = __builtin_amdgcn_readfirstlane(tid >> 6), r = lane >> 4, c = lane & 15;
  const int ch0 = 27 * (h & 7) + 7 * (h >> 3), nch = (h >> 3) == 3 ? 6 : 7;
  const rsrc_t rw = __builtin_amdgcn_make_buffer_rsrc((void*)a.ws, 0, WS_END, RS3);

  if (w < 4) {
    __builtin_amdgcn_s_setprio(2);
    Stream s;
    s.rk = __builtin_amdgcn_make_buffer_rsrc((void*)(a.kc + (size_t)bid * SK * DH), 0, SK * DH * 2, RS3);
    s.rv = __builtin_amdgcn_make_buffer_rsrc((void*)(a.vc + (size_t)bid * SK * DH), 0, SK * DH * 2, RS3);
    s.rd = __builtin_amdgcn_make_buffer_rsrc((void*)(a.wd + (size_t)ch0 * 8 * D), 0, NO_W ? 0 : nch * 8 * D * 2, RS3);
    s.voff = w * 1024 + lane * 16;
    const int kb = w & 1, nb0 = (w >> 1) * 3;
    s.doff = (32 * kb + 8 * r) * (D * 2) + nb0 * 256 + c * 16;
    s.doff2 = w < 2 ? s.doff : OOB - 65536;
    s.c = c;
#pragma unroll
    for (int k = 0; k < 8; ++k) s.acc[k] = 0.f;
#pragma unroll
    for (int k = 0; k < 16; ++k) s.sc[k] = 0.f;
    s.st = st;
    s.fill<0>();
    STAMP(1);
    WG_BARRIER();
    {
      const u32x4 qq = *(const LAS u32x4*)(L + L_Q + c * 16);
#pragma unroll
      for (int k = 0; k < 4; ++k) s.q[k] = qq[k];
    }
    const unsigned tag = __builtin_amdgcn_readfirstlane(*(const LAS unsigned*)(L + L_TAG));
    SB();
    PST(6); VST(1);
    s.first<0>();
    s.rotate();
    s.second<0>();
    u32x4 df[24];
    to_frags(s.draw[0], df); to_frags(s.draw[1], df + 8); to_frags(s.draw[2], df + 16);
#pragma unroll
    for (int k = 0; k < 24; ++k) asm volatile("" : "+v"(df[k]));
    SB();
    for (int n0 = 64; n0 < 224; n0 += 32) { if (n0 == 128) VST(2); s.rotate(); s.steady<0, 0>(n0); }
    s.rotate();
    s.steady<0, 1>(224);
    VST(3);
    s.softmax();
    SB();
    for (int n0 = 256; n0 < 512; n0 += 32) { if (n0 == 320) VST(4); if (n0 == 384) VST(5); if (n0 == 448) VST(6); s.steady<0, 2>(n0); s.rotate(); }
    VST(7);
    {
      const float a0 = swap16_add(s.acc[0], s.acc[1]), a1 = swap16_add(s.acc[2], s.acc[3]);
      const float a2 = swap16_add(s.acc[4], s.acc[5]), a3 = swap16_add(s.acc[6], s.acc[7]);
      const float b0 = swap32_add(a0, a1), b1 = swap32_add(a2, a3);
      const float Ls = wave_sum(s.l);
      LAS float* pa = (LAS float*)(L + L_PA) + w * 128 + 8 * c + r;
      pa[0] = b0; pa[4] = b1;
      if (lane == 0) { ((LAS float*)(L + L_PML))[w] = s.m; ((LAS float*)(L + L_PML))[4 + w] = Ls; }
    }
    STAMP(2);
    WG_BARRIER();
    u32x4 gf[20];
    float hres[3] = {0.f, 0.f, 0.f};
    const int u = lane & 7;
    if (w != 0) gu_frags(L, 3 + w, lane, gf);
    else {
#pragma unroll
      for (int k = 0; k < 20; ++k) gf[k] = u32x4{0u, 0u, 0u, 0u};
      const rsrc_t rx = __builtin_amdgcn_make_buffer_rsrc((void*)(a.x + b * D + 20 * h), 0, 40, RS3);
#pragma unroll
      for (int e = 0; e < 3; ++e) hres[e] = bf2f(__builtin_amdgcn_raw_buffer_load_b16(rx, (3 * u + e) * 2, 0, 0));
    }
    WG_BARRIER();
    STAMP(3); TST(0);
    if (w == 0) {
      float sa[3];
      if (DELAY_A) __builtin_amdgcn_s_sleep(DELAY_A);
      gather_cols<true>(rw, WS_PA, b, h, lane, tag, sa);
      STAMP(4); TST(1);
#pragma unroll
      for (int e = 0; e < 3; ++e) hres[e] = rbf(hres[e] + rbf(sa[e]));
      __builtin_amdgcn_raw_buffer_store_b128(u32x4{f2u(hres[0]), f2u(hres[1]), f2u(hres[2]), tag + 1u}, rw,
                                             lane < 8 ? WS_HB + b * HB_STRIDE + h * 128 + lane * 16 : OOB, 0, SC1);
      if (DELAY_B) __builtin_amdgcn_s_sleep(DELAY_B);
      TST(2);
      u32x4 v[4];
      sweep<4, false>(rw, WS_HB + b * HB_STRIDE + lane * 16, tag + 1u, v);
      STAMP(5); TST(3);
      float sum = 0.f;
#pragma unroll
      for (int i = 0; i < 4; ++i) sum += (u2f(v[i][0]) + u2f(v[i][1])) + u2f(v[i][2]);
      const float mean = wave_sum(sum) * (1.f / D);
      float v01 = 0.f, v2 = 0.f;
#pragma unroll
      for (int i = 0; i < 4; ++i) {
        const float d0 = u2f(v[i][0]) - mean, d1 = u2f(v[i][1]) - mean, d2 = u2f(v[i][2]) - mean;
        v01 = fmaf(d0, d0, v01); v01 = fmaf(d1, d1, v01); v2 = fmaf(d2, d2, v2);
      }
      const float var = wave_sum((u < 7 ? v01 : 0.f) + (u < 6 ? v2 : 0.f)) * (1.f / D);
      const float mb = rbf(mean), ri = __builtin_amdgcn_rcpf(rbf(__builtin_amdgcn_sqrtf(rbf(rbf(var) + 1e-5f))));
      LAS unsigned short* hn = (LAS unsigned short*)(L + L_HN) + 20 * (lane >> 3) + 3 * u;
      LAS unsigned short* dump = (LAS unsigned short*)(L + L_PA);
      LAS unsigned short* h01 = u < 7 ? hn : dump;
      LAS unsigned short* h2 = u < 6 ? hn : dump;
#pragma unroll
      for (int i = 0; i < 4; ++i) {
        h01[160 * i] = f2bf(rbf(u2f(v[i][0]) - mb) * ri);
        h01[160 * i + 1] = f2bf(rbf(u2f(v[i][1]) - mb) * ri);
        h2[160 * i + 2] = f2bf(rbf(u2f(v[i][2]) - mb) * ri);
      }
      TST(4);
    }
    WG_BARRIER();
    if (w != 0) gu_tile(L, 3 + w, lane, gf);
    WG_BARRIER();
    TST(5);
    {
      const u32x4 af = *(const LAS u32x4*)(L + L_ACT + kb * 64 + r * 16);
      if (w < 2) gemv_blocks<3>(af, df, L + L_RB + kb * 2560, lane);
      else gemv_blocks<2>(af, df, L + L_RB + kb * 2560 + 3 * 512, lane);
    }
    TST(6);
    WG_BARRIER();
    STAMP(6);
    if (w == 0) {
      float sc[3];
      if (DELAY_C) __builtin_amdgcn_s_sleep(DELAY_C);
      gather_cols<false>(rw, WS_PC, b, h, lane, tag + 1u, sc);
      TST(7);
      const rsrc_t ry = __builtin_amdgcn_make_buffer_rsrc((void*)(a.out + b * D + 20 * h), 0, 40, RS3);
#pragma unroll
      for (int e = 0; e < 3; ++e) __builtin_amdgcn_raw_buffer_store_b16(f2bf(hres[e] + rbf(sc[e])), ry, lane < 8 ? (3 * u + e) * 2 : OOB, 0, 0);
      if (bid == 0 && lane == 0) __hip_atomic_store((GAS unsigned*)(a.ws + WS_SEED), tag + 1u, __ATOMIC_RELAXED, __HIP_MEMORY_SCOPE_AGENT);
    }
  } else {
    const int hw = w - 4;
    if (hw == 0) {
      const int hq = bid >> 3, ks = bid & 7;
      const unsigned seed = __builtin_amdgcn_raw_buffer_load_b32(rw, WS_SEED, 0, SC1);
      const rsrc_t rx = __builtin_amdgcn_make_buffer_rsrc((void*)a.x, 0, B * D * 2, RS3);
      const rsrc_t rq = __builtin_amdgcn_make_buffer_rsrc((void*)(a.wq + hq * DH), 0, (80 * (ks + 1) - 1) * (H * DH * 2) + DH * 2, RS3);
      u32x4 xs[10], xa[3], raw[3][8];
      const int xrow = (c & 7) * (D * 2);
#pragma unroll
      for (int i = 0; i < 10; ++i) xs[i] = __builtin_amdgcn_raw_buffer_load_b128(rx, (lane >> 3) * (D * 2) + (lane & 7) * 16 + i * 128, 0, 0);
#pragma unroll
      for (int k = 0; k < 3; ++k) xa[k] = __builtin_amdgcn_raw_buffer_load_b128(rx, (32 * k + 8 * r < 80) ? xrow + (80 * ks + 32 * k + 8 * r) * 2 : OOB, 0, 0);
      SB();
#pragma unroll
      for (int k = 0; k < 3; ++k)
#pragma unroll
        for (int i = 0; i < 8; ++i)
          raw[k][i] = __builtin_amdgcn_raw_buffer_load_b128(rq, 8 * r * (H * DH * 2) + c * 16, (80 * ks + 32 * k + i) * (H * DH * 2), W_AUX);
      SB();
      PST(1);
      float sum = 0.f, ssq = 0.f;
#pragma unroll
      for (int i = 0; i < 10; ++i)
#pragma unroll
        for (int k = 0; k < 4; ++k) { sum = dot2(xs[i][k], 0x3f803f80u, sum); ssq = dot2(xs[i][k], xs[i][k], ssq); }
      sum += dpp<XOR1>(sum); ssq += dpp<XOR1>(ssq); sum += dpp<XOR2>(sum); ssq += dpp<XOR2>(ssq); sum += dpp<XOR7>(sum); ssq += dpp<XOR7>(ssq);
      const float mean = sum * (1.f / D);
      const float var = fmaxf(fmaf(-mean, mean, ssq * (1.f / D)), 0.f);
      PST(2);
      const float mb = u2f(__builtin_amdgcn_ds_bpermute((c & 7) * 32, f2u(rbf(mean))));
      const float ri = u2f(__builtin_amdgcn_ds_bpermute((c & 7) * 32, f2u(__builtin_amdgcn_rcpf(rbf(__builtin_amdgcn_sqrtf(rbf(rbf(var) + 1e-5f)))))));
      PST(3);
      f32x4 d[8];
#pragma unroll
      for (int e = 0; e < 8; ++e) d[e] = f32x4{0.f, 0.f, 0.f, 0.f};
#pragma unroll
      for (int k = 0; k < 3; ++k) {
        u32x4 af;
#pragma unroll
        for (int i = 0; i < 4; ++i) af[i] = (32 * k + 8 * r < 80) ? pk2(rbf(bf_lo(xa[k][i]) - mb) * ri, rbf(bf_hi(xa[k][i]) - mb) * ri) : 0u;
        u32x4 fr[8];
        to_frags(raw[k], fr);
#pragma unroll
        for (int e = 0; e < 8; ++e) d[e] = mfma(af, fr[e], d[e]);
      }
      PST(4);
      const unsigned tag = __builtin_amdgcn_readfirstlane(seed) + 1u;
      const bool up = r >= 2;
#pragma unroll
      for (int i = 0; i < 4; ++i) {
        const int off = WS_QP + ((hq * 8 + ((4 * r + i) & 7)) * 8 + ks) * 1024 + (c + (up ? 16 : 0)) * 16;
        const float e0 = up ? d[4][i] : d[0][i], e1 = up ? d[5][i] : d[1][i], e2 = up ? d[6][i] : d[2][i], e3 = up ? d[7][i] : d[3][i];
        __builtin_amdgcn_raw_buffer_store_b128(u32x4{f2u(e0), tag, f2u(e1), tag}, rw, off, 0, SC1);
        __builtin_amdgcn_raw_buffer_store_b128(u32x4{f2u(e2), tag, f2u(e3), tag}, rw, off + 512, 0, SC1);
      }
      STAMP(4); PST(5);
      for (int spin = 0; spin < (1 << 20); ++spin) {
        __builtin_amdgcn_s_sleep(QPOLL_SLEEP);
        const u32x4 t = __builtin_amdgcn_raw_buffer_load_b128(rw, WS_QP + (h * 8 + b) * 8192 + (lane >> 3) * 1024 + 896 + (lane & 7) * 16, 0, SC1);
        if (__builtin_amdgcn_ballot_w64(t[3] != tag) == 0ull) break;
        CBAR();
      }
      u32x4 v[8];
      sweep<8, false, true>(rw, WS_QP + (h * 8 + b) * 8192 + lane * 16, tag, v);
      PST(6);
      float q0 = 0.f, q1 = 0.f;
#pragma unroll
      for (int i = 0; i < 8; ++i) { q0 += u2f(v[i][0]); q1 += u2f(v[i][2]); }
      ((LAS unsigned*)(L + L_Q))[4 * c + 2 * (r & 1) + (r >> 1)] = pk2(q0, q1);
      if (lane == 0) *(LAS unsigned*)(L + L_TAG) = tag;
      if (lane < 4) ((LAS unsigned*)(L + L_ACT))[28 + lane] = 0u;
    }
    STAMP(1);
    WG_BARRIER();
    const unsigned tag = __builtin_amdgcn_readfirstlane(*(const LAS unsigned*)(L + L_TAG));
    u32x4 of[40];
    {
      const rsrc_t ro = __builtin_amdgcn_make_buffer_rsrc((void*)(a.wo + (size_t)h * DH * D), 0, NO_W ? 0 : DH * D * 2, RS3);
      const rsrc_t rg = __builtin_amdgcn_make_buffer_rsrc((void*)(hw < 2 ? a.wg : a.wu), 0, NO_W ? 0 : D * F * 2, RS3);
      const int ooff = (32 * hw + 8 * r) * (D * 2) + c * 16;
      int id = (hw & 1) * 35 * 64 + lane;
      int row = id / 7, pc = id - 7 * row;
      LAS unsigned char* dst = L + (hw < 2 ? L_G : L_U) + (hw & 1) * 35 * 1024;
#pragma unroll
      for (int n = 0; n < 5; ++n) {
        u32x4 raw[8];
#pragma unroll
        for (int i = 0; i < 8; ++i) {
          raw[i] = __builtin_amdgcn_raw_buffer_load_b128(ro, ooff + n * 256, i * (D * 2), W_AUX);
          CBAR(); __builtin_amdgcn_s_sleep(HSLEEP); CBAR();
        }
        to_frags(raw, of + 8 * n);
#pragma unroll
        for (int e = 0; e < 8; ++e) asm volatile("" : "+v"(of[8 * n + e]));
        for (int i = 0; i < 7; ++i) {
          __builtin_amdgcn_raw_ptr_buffer_load_lds(rg, (LAS void*)dst, 16, pc < nch ? row * (F * 2) + (ch0 + pc) * 16 : OOB, 0, 0, W_AUX);
          dst += 1024;
          row += 9; pc += 1;
          if (pc >= 7) { pc -= 7; row += 1; }
          CBAR(); __builtin_amdgcn_s_sleep(HSLEEP); CBAR();
        }
      }
    }
    asm volatile("s_waitcnt vmcnt(0)" ::: "memory");
    STAMP(2);
    WG_BARRIER();
    TST(0);
    {
      const f32x4 mm = *(const LAS f32x4*)(L + L_PML), ll = *(const LAS f32x4*)(L + L_PML + 16);
      f32x4 pv[4][2];
#pragma unroll
      for (int ww = 0; ww < 4; ++ww) {
        const LAS f32x4* p = (const LAS f32x4*)(L + L_PA + (ww * 128 + 32 * hw + 8 * r) * 4);
        pv[ww][0] = p[0]; pv[ww][1] = p[1];
      }
      const float M = fmaxf(fmaxf(mm[0], mm[1]), fmaxf(mm[2], mm[3]));
      float wt[4], Ls = 0.f;
#pragma unroll
      for (int ww = 0; ww < 4; ++ww) { wt[ww] = __builtin_amdgcn_exp2f((mm[ww] - M) * LOG2E); Ls = fmaf(wt[ww], ll[ww], Ls); }
      const float inv = __builtin_amdgcn_rcpf(Ls);
      f32x4 o0 = pv[0][0] * wt[0], o1 = pv[0][1] * wt[0];
#pragma unroll
      for (int ww = 1; ww < 4; ++ww) { o0 += pv[ww][0] * wt[ww]; o1 += pv[ww][1] * wt[ww]; }
      o0 *= inv; o1 *= inv;
      const u32x4 af = {pk2(o0[0], o0[1]), pk2(o0[2], o0[3]), pk2(o1[0], o1[1]), pk2(o1[2], o1[3])};
      TST(1);
      gemv_blocks<5>(af, of, L + L_RB + hw * 2560, lane);
      TST(2);
    }
    WG_BARRIER();
    TST(3);
    publish_cols<4>(L, rw, WS_PA, b, h, tid - 256, tag);
    STAMP(3); TST(4);
    u32x4 gf[20];
    gu_frags(L, hw, lane, gf);
    WG_BARRIER();
    TST(5);
    gu_tile(L, hw, lane, gf);
    STAMP(5); TST(6);
    WG_BARRIER();
    WG_BARRIER();
    publish_cols<2>(L, rw, WS_PC, b, h, tid - 256, tag + 1u);
    STAMP(6); TST(7);
  }
  STAMP(7); PST(7);
}

__global__ void k_first(unsigned char* ws) { if (threadIdx.x == 0) *(unsigned long long*)(ws + WS_STAMPS) = __builtin_amdgcn_s_memrealtime(); }
__global__ void k_last(unsigned char* ws) { const unsigned long long t = *(volatile unsigned long long*)(ws + WS_STAMPS); const unsigned long long n = __builtin_amdgcn_s_memrealtime(); if (n - t < 30000) while (__builtin_amdgcn_s_memrealtime() < t + 30000) __builtin_amdgcn_s_sleep(8); }
}

extern "C" void kernel_launch(void* const* d_in, const int* in_sizes, int n_in, void* d_out, int out_size, void* d_ws,
                              size_t ws_size, hipStream_t stream) {
  Args a;
  a.x = (const unsigned short*)d_in[0];
  a.kc = (const unsigned short*)d_in[1];
  a.vc = (const unsigned short*)d_in[2];
  a.wq = (const unsigned short*)d_in[3];
  a.wo = (const unsigned short*)d_in[4];
  a.wg = (const unsigned short*)d_in[6];
  a.wu = (const unsigned short*)d_in[7];
  a.wd = (const unsigned short*)d_in[8];
  a.out = (unsigned short*)d_out;
  a.ws = (unsigned char*)d_ws;
  k_first<<<1, 64, 0, stream>>>(a.ws);
  k_block<<<B * H, 512, 0, stream>>>(a);
  k_last<<<1, 64, 0, stream>>>(a.ws);
}
```

```cpp
#include <hip/hip_runtime.h>
#include <hip/hip_bf16.h>
#include <stdint.h>

#define STAMPS 0
#define RDEPTH 5
#define W_AUX 0
#define EXT 0
#define GEMV_N 1
#define DEEP0 0
#define CS_DELAY 6
#define HSLEEP 26
#define DELAY_Q 40
#define DELAY_S 0
#define DELAY_A 18
#define DELAY_B 12
#define DELAY_C 20
#define POLL_SLEEP_FAR 12
#define POLL_FAR 16
#define TS_SLEEP 24
#define SP_WAIT 2
#define R2 6
#define DEEP_AT 64
#define REMAP 0
#define ROT 1
#define DEEP_PAR 0
#define DEEP_TWO 48
#define ADVICE 0
#define TS_TRIES 16
#define LAG0 50.f
#define GAIN 0.6f
#define CK1 96
#define BETA 0.5f
#define GAMMA 1.0f

namespace {
constexpr int B = 8, D = 640, H = 32, SK = 4096, DH = 128, F = 1728;
typedef unsigned u32x4 __attribute__((ext_vector_type(4)));
typedef unsigned u32x2 __attribute__((ext_vector_type(2)));
typedef float f32x4 __attribute__((ext_vector_type(4)));
typedef float f32x2 __attribute__((ext_vector_type(2)));
typedef short s16x4 __attribute__((ext_vector_type(4)));
typedef __bf16 bf16x2 __attribute__((ext_vector_type(2)));
typedef __bf16 bf16x8 __attribute__((ext_vector_type(8)));
#define LAS __attribute__((address_space(3)))
#define GAS __attribute__((address_space(1)))
typedef __amdgpu_buffer_rsrc_t rsrc_t;

__device__ __forceinline__ float u2f(unsigned u) { return __builtin_bit_cast(float, u); }
__device__ __forceinline__ unsigned f2u(float f) { return __builtin_bit_cast(unsigned, f); }
__device__ __forceinline__ float bf_lo(unsigned u) { return u2f(u << 16); }
__device__ __forceinline__ float bf_hi(unsigned u) { return u2f(u & 0xffff0000u); }
__device__ __forceinline__ float bf2f(unsigned short u) { return u2f((unsigned)u << 16); }
__device__ __forceinline__ unsigned short f2bf(float f) { return __builtin_bit_cast(unsigned short, (__bf16)f); }
__device__ __forceinline__ float rbf(float f) { return bf2f(f2bf(f)); }
__device__ __forceinline__ unsigned pk2(float lo, float hi) {
  bf16x2 v = {(__bf16)lo, (__bf16)hi};
  return __builtin_bit_cast(unsigned, v);
}
__device__ __forceinline__ float dot2(unsigned k, unsigned q, float acc) {
  return __builtin_amdgcn_fdot2_f32_bf16(__builtin_bit_cast(bf16x2, k), __builtin_bit_cast(bf16x2, q), acc, false);
}
__device__ __forceinline__ f32x4 mfma(u32x4 a, u32x4 b, f32x4 c) {
  return __builtin_amdgcn_mfma_f32_16x16x32_bf16(__builtin_bit_cast(bf16x8, a), __builtin_bit_cast(bf16x8, b), c, 0, 0, 0);
}
template <int CTRL> __device__ __forceinline__ float dpp(float x) {
  return __builtin_bit_cast(float, __builtin_amdgcn_mov_dpp(__builtin_bit_cast(int, x), CTRL, 0xf, 0xf, true));
}
constexpr int XOR1 = 0xB1, XOR2 = 0x4E, XOR7 = 0x141, XOR8 = 0x128;
__device__ __forceinline__ float row16_sum(float x) { x += dpp<XOR1>(x); x += dpp<XOR2>(x); x += dpp<XOR7>(x); x += dpp<XOR8>(x); return x; }
__device__ __forceinline__ float row16_max(float x) {
  x = fmaxf(x, dpp<XOR1>(x)); x = fmaxf(x, dpp<XOR2>(x)); x = fmaxf(x, dpp<XOR7>(x)); x = fmaxf(x, dpp<XOR8>(x)); return x;
}
__device__ __forceinline__ float swap16_add(float a, float b) {
  auto s = __builtin_amdgcn_permlane16_swap(f2u(a), f2u(b), false, false);
  return u2f(s[0]) + u2f(s[1]);
}
__device__ __forceinline__ float swap32_add(float a, float b) {
  auto s = __builtin_amdgcn_permlane32_swap(f2u(a), f2u(b), false, false);
  return u2f(s[0]) + u2f(s[1]);
}
__device__ __forceinline__ float xrow_sum(float x) { x = swap16_add(x, x); return swap32_add(x, x); }
__device__ __forceinline__ float xrow_max(float x) {
  auto s = __builtin_amdgcn_permlane16_swap(f2u(x), f2u(x), false, false);
  x = fmaxf(u2f(s[0]), u2f(s[1]));
  auto t = __builtin_amdgcn_permlane32_swap(f2u(x), f2u(x), false, false);
  return fmaxf(u2f(t[0]), u2f(t[1]));
}
__device__ __forceinline__ float wave_sum(float x) { return xrow_sum(row16_sum(x)); }
__device__ __forceinline__ float inv_std(float var) { return __builtin_amdgcn_rcpf(rbf(__builtin_amdgcn_sqrtf(rbf(rbf(var) + 1e-5f)))); }
__device__ __forceinline__ f32x2 fold_rows(const float (&a)[8]) {
  const float a0 = swap16_add(a[0], a[1]), a1 = swap16_add(a[2], a[3]), a2 = swap16_add(a[4], a[5]), a3 = swap16_add(a[6], a[7]);
  return f32x2{swap32_add(a0, a1), swap32_add(a2, a3)};
}

#define SB() __builtin_amdgcn_sched_barrier(0)
#define CBAR() asm volatile("" ::: "memory")
#define WG_BARRIER() do { asm volatile("s_waitcnt lgkmcnt(0)" ::: "memory"); __builtin_amdgcn_s_barrier(); CBAR(); } while (0)
#define ST(kind, i)

constexpr int R = RDEPTH;
constexpr int NT = 2, SC1 = 16;
constexpr int RS3 = 0x00020000;
constexpr int OOB = 0x7ff00000;
constexpr int SPIN = 1 << 20;
constexpr float SCALE = 0.08838834764831845f, LOG2E = 1.4426950408889634f;

constexpr int WS_SEED = 0;
constexpr int WS_QP = 4096;
constexpr int WS_PA = WS_QP + 32 * 8 * 8 * 512;
constexpr int WS_TS = WS_PA + 8 * 32 * 32 * 128;
constexpr int WS_END = WS_TS + 256 * 16;
constexpr int WS_HB = WS_QP, HB_STRIDE = 4096;
constexpr int WS_PC = WS_PA;
constexpr int WS_STAMPS = 16 << 20;
constexpr int L_G = 0, L_U = 640 * 112, L_RB = 2 * 640 * 112;
constexpr int L_P = L_RB, L_PS = L_RB + 8192;
constexpr int L_PA = L_RB + 4 * 640 * 4;
constexpr int L_PML = L_PA + 5 * 128 * 4;
constexpr int L_DEC = L_PML + 64;
constexpr int L_TS = L_DEC + 16;
constexpr int L_LN = L_TS + 16;
constexpr int L_Q = L_LN + 16;
constexpr int L_TAG = L_Q + 256;
constexpr int L_HN = L_TAG + 16;
constexpr int L_ACT = L_HN + 1280;
constexpr int L_END = L_ACT + 128;
static_assert(L_END <= 163840, "LDS");

__device__ __forceinline__ void to_frags(const u32x4 (&raw)[8], u32x4* fr) {
#pragma unroll
  for (int d = 0; d < 4; ++d) {
    u32x4 lo, hi;
#pragma unroll
    for (int kp = 0; kp < 4; ++kp) {
      lo[kp] = __builtin_amdgcn_perm(raw[2 * kp + 1][d], raw[2 * kp][d], 0x05040100u);
      hi[kp] = __builtin_amdgcn_perm(raw[2 * kp + 1][d], raw[2 * kp][d], 0x07060302u);
    }
    fr[2 * d] = lo; fr[2 * d + 1] = hi;
  }
}

template <int KIND, int FARSLEEP, int SLEEP> __device__ __forceinline__ void sweep(rsrc_t rs, int voff, unsigned tag, u32x4 (&v)[4]) {
  for (int spin = 0; spin < SPIN; ++spin) {
#pragma unroll
    for (int i = 0; i < 4; ++i) v[i] = __builtin_amdgcn_raw_buffer_load_b128(rs, voff + i * 1024, 0, SC1);
    unsigned bad = 0u;
#pragma unroll
    for (int i = 0; i < 4; ++i)
      bad |= (KIND == 0 ? v[i][3] : (__builtin_amdgcn_perm(v[i][1], v[i][0], 0x0c0c0400u) | __builtin_amdgcn_perm(v[i][3], v[i][2], 0x04000c0cu))) ^ tag;
    const unsigned long long m = __builtin_amdgcn_ballot_w64(bad != 0u);
    if (m == 0ull) break;
    if (FARSLEEP && __builtin_popcountll(m) > POLL_FAR) __builtin_amdgcn_s_sleep(FARSLEEP);
    else if (SLEEP) __builtin_amdgcn_s_sleep(SLEEP);
    CBAR();
  }
}
__device__ __forceinline__ f32x4 wave_factors(LAS unsigned char* L) {
  u32x4 e[4];
  for (int spin = 0; spin < SPIN; ++spin) {
#pragma unroll
    for (int k = 0; k < 4; ++k) e[k] = *(volatile LAS u32x4*)(L + L_PML + k * 16);
    if (__builtin_amdgcn_readfirstlane(e[0][2] & e[1][2] & e[2][2] & e[3][2])) break;
    __builtin_amdgcn_s_sleep(8);
  }
  const float M = fmaxf(fmaxf(u2f(e[0][0]), u2f(e[1][0])), fmaxf(u2f(e[2][0]), u2f(e[3][0])));
  float Ls = 0.f;
  f32x4 t;
#pragma unroll
  for (int k = 0; k < 4; ++k) { t[k] = __builtin_amdgcn_exp2f((u2f(e[k][0]) - M) * LOG2E); Ls = fmaf(t[k], u2f(e[k][1]), Ls); }
  return t * (1.f / Ls);
}

struct Stream {
  rsrc_t rk, rv, rd;
  int voff, doff, doff2, vbase;
  u32x4 ring[32];
  u32x4 ext[EXT > 0 ? EXT : 1];
  u32x4 draw[3][8];
  unsigned q[4];
  float x[16];
  float sc[16];
  float acc[8];
  int c;

  template <int J, bool LAST> __device__ __forceinline__ void issue(int n0) {
    if constexpr (!LAST || J < 32) ring[J & 31] = __builtin_amdgcn_raw_buffer_load_b128(rk, voff, (n0 + J) * 4096, NT);
    else ring[J & 31] = __builtin_amdgcn_raw_buffer_load_b128(rv, voff, vbase + (J - 32) * 4096, NT);
  }
  template <int CTRL, int BIT> __device__ __forceinline__ float tr(float lo, float hi) {
    const float t1 = lo + dpp<CTRL>(lo), t2 = hi + dpp<CTRL>(hi);
    return (c & BIT) ? t2 : t1;
  }
  template <int J> __device__ __forceinline__ void consume_k() {
    constexpr int I = J & 15;
    float d = dot2(ring[J][0], q[0], 0.f);
    d = dot2(ring[J][1], q[1], d); d = dot2(ring[J][2], q[2], d); d = dot2(ring[J][3], q[3], d);
    x[I] = d;
    if constexpr ((I & 1) == 1) x[I] = tr<XOR8, 8>(x[I - 1], x[I]);
    if constexpr ((I & 3) == 3) x[I] = tr<XOR7, 4>(x[I - 2], x[I]);
    if constexpr ((I & 7) == 7) x[I] = tr<XOR2, 2>(x[I - 4], x[I]);
    if constexpr (I == 15) x[I] = rbf(rbf(tr<XOR1, 1>(x[I - 8], x[I])) * SCALE);
    asm volatile("" : "+v"(x[I]));
    if constexpr (J == 15) sc[14] = x[15];
    if constexpr (J == 31) sc[15] = x[15];
  }
  __device__ __forceinline__ void rotate() {
#pragma unroll
    for (int k = 0; k < 14; ++k) sc[k] = sc[k + 2];
  }
  template <int J> __device__ __forceinline__ void fill() {
    if constexpr (J < 32 + EXT) {
      asm volatile("s_waitcnt vmcnt(%0)" :: "n"(R - 1) : "memory");
      if constexpr (J == R || J == 8 || J == 16 || J == 24 || J == 31 + EXT) ST(2, J == R ? 1 : J == 31 + EXT ? 5 : J / 8 + 1);
      if constexpr (J < 32) issue<J, false>(0);
      else ext[J - 32] = __builtin_amdgcn_raw_buffer_load_b128(rk, voff, J * 4096, NT);
      SB();
      fill<J + 1>();
    }
  }
  static constexpr int N1 = (32 - EXT) / 2, NI2 = 32 + R - EXT - N1;
  template <int J> __device__ __forceinline__ void first() {
    if constexpr (J < 32) {
      consume_k<J>();
      if constexpr (J < EXT) ring[J] = ext[J];
      if constexpr ((J & 1) && J > EXT) {
        asm volatile("s_waitcnt vmcnt(%0)" :: "n"(R - 1) : "memory");
        issue<32 + EXT + (J - EXT) / 2, false>(0);
      }
      SB();
      first<J + 1>();
    }
  }
  static constexpr bool issues(int J) { return (J + 1) * NI2 / 32 > J * NI2 / 32; }
  static constexpr int pace2(int J) {
    int kv = 0, n = 0;
    for (int t = J - 1; t >= 0; --t) {
      if (t < 24) ++n;
      if (issues(t)) { if (++kv == R) return n; ++n; }
    }
    return n + (R - 1 - kv);
  }
  template <int J> __device__ __forceinline__ void second() {
    if constexpr (J < 32) {
      if constexpr (issues(J)) {
        asm volatile("s_waitcnt vmcnt(%0)" :: "n"(pace2(J)) : "memory");
        issue<EXT + N1 + J * NI2 / 32, false>(32);
        SB();
      }
      if constexpr (J < 24) draw[J / 8][J % 8] = __builtin_amdgcn_raw_buffer_load_b128(rd, J < 16 ? doff : doff2, (J % 8) * (D * 2) + (J / 8) * 256, W_AUX);
      SB();
      consume_k<J>();
      SB();
      second<J + 1>();
    }
  }
  template <int J, bool LAST> __device__ __forceinline__ void steady(int n0) {
    if constexpr (J < 32) {
      asm volatile("s_waitcnt vmcnt(%0)" :: "n"(R - 1) : "memory");
      issue<J + R, LAST>(n0);
      SB();
      consume_k<J>();
      SB();
      steady<J + 1, LAST>(n0);
    }
  }

  const LAS unsigned char* pb;
  unsigned short pr[8];
  int ioff, poff;
  template <int SLOT> __device__ __forceinline__ void vissue() {
    ring[SLOT] = __builtin_amdgcn_raw_buffer_load_b128(rv, voff, ioff, NT);
    pr[SLOT] = *(const LAS unsigned short*)(pb + poff);
    ioff += 4096; poff += 8;
    SB();
  }
  template <int S, int RR> __device__ __forceinline__ void vround() {
    if constexpr (S < 8) {
      asm volatile("s_waitcnt vmcnt(%0)" :: "n"(RR - 1) : "memory");
      vissue<(S + RR) & 7>();
      const float p = bf2f(pr[S]);
      f32x2 t[4];
#pragma unroll
      for (int k = 0; k < 4; ++k) t[k] = __builtin_elementwise_fma(f32x2{p, p}, f32x2{bf_lo(ring[S][k]), bf_hi(ring[S][k])}, f32x2{acc[2 * k], acc[2 * k + 1]});
      asm volatile("" : "+v"(t[0]), "+v"(t[1]), "+v"(t[2]), "+v"(t[3]));
#pragma unroll
      for (int k = 0; k < 4; ++k) { acc[2 * k] = t[k][0]; acc[2 * k + 1] = t[k][1]; }
      SB();
      vround<S + 1, RR>();
    }
  }
  template <int J, int RR> __device__ __forceinline__ void vdeepen() {
    if constexpr (J < RR) { vissue<J>(); vdeepen<J + 1, RR>(); }
  }
};

struct Args {
  const unsigned short *kc, *x, *wq;
  unsigned char* ws;
  const unsigned short *vc, *wo, *wg, *wu, *wd;
  unsigned short* out;
};

__device__ __forceinline__ void gu_frags(LAS unsigned char* L, int tile, int lane, u32x4 (&gf)[20]) {
  const int g = lane >> 4, q = (lane >> 2) & 3, p = lane & 3;
  LAS unsigned char* a = L + (p < 2 ? L_G : L_U) + (8 * g + q) * 112 + tile * 16 + (p & 1) * 8;
#pragma unroll
  for (int s = 0; s < 20; ++s) {
    const s16x4 t0 = __builtin_amdgcn_ds_read_tr16_b64_v4i16((LAS s16x4*)(a + s * 3584));
    const s16x4 t1 = __builtin_amdgcn_ds_read_tr16_b64_v4i16((LAS s16x4*)(a + s * 3584 + 448));
    const u32x2 u0 = __builtin_bit_cast(u32x2, t0), u1 = __builtin_bit_cast(u32x2, t1);
    gf[s] = u32x4{u0[0], u0[1], u1[0], u1[1]};
  }
}
__device__ __forceinline__ float gu_colsum(const u32x4 (&gf)[20]) {
  f32x4 cs = {0.f, 0.f, 0.f, 0.f};
#pragma unroll
  for (int s = 0; s < 20; ++s) cs = mfma(u32x4{0x3f803f80u, 0x3f803f80u, 0x3f803f80u, 0x3f803f80u}, gf[s], cs);
  float r = cs[0];
  asm volatile("" : "+v"(r));
  return r;
}
__device__ __forceinline__ void gu_tile(LAS unsigned char* L, int tile, int lane, const u32x4 (&gf)[20], float cs) {
  f32x4 acc = {0.f, 0.f, 0.f, 0.f};
  const LAS u32x4* hn = (const LAS u32x4*)(L + L_HN) + (lane >> 4);
#pragma unroll
  for (int s = 0; s < 20; ++s) acc = mfma(hn[4 * s], gf[s], acc);
  u32x4 ln = *(volatile LAS u32x4*)(L + L_LN);
  for (int spin = 0; spin < SPIN && __builtin_amdgcn_readfirstlane(ln[2]) == 0u; ++spin) ln = *(volatile LAS u32x4*)(L + L_LN);
  const float y = rbf(fmaf(-u2f(ln[0]), cs, acc[0]) * u2f(ln[1]));
  const float g = y, u = dpp<XOR8>(y);
  const float z = 0.7978845608028654f * (g + 0.044715f * g * g * g);
  const float e = __builtin_amdgcn_exp2f(z * (2.f * LOG2E));
  const float th = 1.f - 2.f * __builtin_amdgcn_rcpf(e + 1.f);
  const float ge = rbf(0.5f * g * (1.f + th));
  if (lane < 8) ((LAS unsigned short*)(L + L_ACT))[tile * 8 + lane] = f2bf(ge * u);
}
template <int NB> __device__ __forceinline__ void gemv_blocks(const u32x4 af, const u32x4* fr, const unsigned (&mk)[8], LAS unsigned char* dst) {
  u32x4 am[8];
#pragma unroll
  for (int e = 0; e < 8; ++e) am[e] = af & mk[e];
#pragma unroll
  for (int n = 0; n < NB; ++n) {
    f32x4 d = {0.f, 0.f, 0.f, 0.f};
#pragma unroll
    for (int e = 0; e < 8; ++e) d = mfma(am[e], fr[n * 8 + e], d);
    *(LAS f32x4*)(dst + n * 512) = d;
  }
}
__device__ __forceinline__ void row_masks(int lane, unsigned (&mk)[8]) {
#pragma unroll
  for (int e = 0; e < 8; ++e) { mk[e] = (lane & 7) == e ? ~0u : 0u; asm volatile("" : "+v"(mk[e])); }
}
template <int NP> __device__ __forceinline__ void publish_cols(const LAS float* p, rsrc_t rw, int off, unsigned m01, unsigned m2, unsigned tag) {
  float s0 = p[0], s1 = p[1], s2 = p[2];
#pragma unroll
  for (int k = 1; k < NP; ++k) { s0 += p[k * 640]; s1 += p[k * 640 + 1]; s2 += p[k * 640 + 2]; }
  __builtin_amdgcn_raw_buffer_store_b128(u32x4{f2u(s0) & m01, f2u(s1) & m01, f2u(s2) & m2, tag}, rw, off, 0, SC1);
}
template <int FARSLEEP> __device__ __forceinline__ void gather_cols(rsrc_t rw, int off, unsigned tag, float (&s)[3]) {
  u32x4 v[4];
  sweep<0, FARSLEEP, 0>(rw, off, tag, v);
#pragma unroll
  for (int e = 0; e < 3; ++e) {
    float x = (u2f(v[0][e]) + u2f(v[1][e])) + (u2f(v[2][e]) + u2f(v[3][e]));
    x += dpp<XOR8>(x);
    s[e] = xrow_sum(x);
  }
}

__global__ void __launch_bounds__(512, 2) k_block(Args a) {
  __shared__ __attribute__((aligned(16))) unsigned char lds_[L_END];
  LAS unsigned char* L = (LAS unsigned char*)lds_;
  ST(1, 0); ST(2, 0); ST(4, 0); ST(5, 0);
  const int bid = REMAP ? (int)((blockIdx.x & 7) | ((31 - (blockIdx.x >> 3)) << 3)) : (int)blockIdx.x, h = bid & 31, b = bid >> 5;
  const int tid = threadIdx.x, lane = tid & 63, w = __builtin_amdgcn_readfirstlane(tid >> 6), r = lane >> 4, c = lane & 15;
  const rsrc_t rk = __builtin_amdgcn_make_buffer_rsrc((void*)(a.kc + (size_t)bid * SK * DH), 0, SK * DH * 2, RS3);
  u32x4 pre[R];
  if (DELAY_S && w < 4) __builtin_amdgcn_s_sleep(DELAY_S);
#pragma unroll
  for (int j = 0; j < R; ++j) pre[j] = __builtin_amdgcn_raw_buffer_load_b128(rk, w < 4 ? w * 1024 + lane * 16 : OOB, j * 4096, NT);
  CBAR(); SB();
  const int ch0 = 27 * (h & 7) + 7 * (h >> 3), nch = (h >> 3) == 3 ? 6 : 7;
  const rsrc_t rw = __builtin_amdgcn_make_buffer_rsrc((void*)a.ws, 0, WS_END, RS3);

  if (w < 4) {
    __builtin_amdgcn_s_setprio(2);
    Stream s;
    s.rk = rk;
    s.rv = __builtin_amdgcn_make_buffer_rsrc((void*)(a.vc + (size_t)bid * SK * DH), 0, SK * DH * 2, RS3);
    s.rd = __builtin_amdgcn_make_buffer_rsrc((void*)(a.wd + (size_t)ch0 * 8 * D), 0, nch * 8 * D * 2, RS3);
    s.voff = w * 1024 + lane * 16;
    s.vbase = 0;
    const int kb = w & 1, nb0 = (w >> 1) * 3;
    s.doff = (32 * kb + 8 * r) * (D * 2) + nb0 * 256 + c * 16;
    s.doff2 = w < 2 ? s.doff : OOB;
    s.c = c;
#pragma unroll
    for (int k = 0; k < 8; ++k) s.acc[k] = 0.f;
#pragma unroll
    for (int k = 0; k < 16; ++k) s.sc[k] = 0.f;
#pragma unroll
    for (int j = 0; j < R; ++j) s.ring[j] = pre[j];
    s.fill<R>();
    ST(1, 1);
    WG_BARRIER();
    {
      const u32x4 qq = *(const LAS u32x4*)(L + L_Q + c * 16);
#pragma unroll
      for (int k = 0; k < 4; ++k) s.q[k] = qq[k];
    }
    const unsigned tag = __builtin_amdgcn_readfirstlane(*(const LAS unsigned*)(L + L_TAG));
    SB();
    ST(2, 6); ST(4, 1);
    s.first<0>();
    s.rotate();
    s.second<0>();
    u32x4 df[24];
    to_frags(s.draw[0], df); to_frags(s.draw[1], df + 8); to_frags(s.draw[2], df + 16);
#pragma unroll
    for (int k = 0; k < 24; ++k) asm volatile("" : "+v"(df[k]));
    SB();
    for (int n0 = 64; n0 < 224; n0 += 32) { s.rotate(); s.steady<0, false>(n0); }
    s.rotate();
    s.steady<0, true>(224);
    ST(4, 2);
    const unsigned tK = (unsigned)__builtin_amdgcn_s_memrealtime();
    {
      float mx = s.sc[0];
#pragma unroll
      for (int k = 1; k < 16; ++k) mx = fmaxf(mx, s.sc[k]);
      const float m = xrow_max(row16_max(mx));
      LAS unsigned short* pw = (LAS unsigned short*)(L + L_P + w * 2048 + (__builtin_bitreverse32(c) >> 28) * 8 + r * 2);
      float l = 0.f;
      unsigned short pk[16];
#pragma unroll
      for (int k = 0; k < 16; ++k) { pk[k] = f2bf(__builtin_amdgcn_exp2f((s.sc[k] - m) * LOG2E)); pw[k * 64] = pk[k]; l += bf2f(pk[k]); }
      s.pb = L + r * 2;
      CBAR();
#pragma unroll
      for (int k = 0; k < R; ++k) s.pr[k] = *(const LAS unsigned short*)(s.pb + L_P + w * 2048 + k * 8);
      l = wave_sum(l);
      if (lane == 0) *(volatile LAS u32x4*)(L + L_PML + w * 16) = u32x4{f2u(m), f2u(l), 1u, 0u};
    }
    s.ioff = s.vbase + R * 4096; s.poff = L_P + w * 2048 + R * 8;
    int rem = 256, deep = 0;
    SB();
    for (; rem >= deep + 8; rem -= 8) {
      if (rem == 256 - CK1 && w == 0) *(volatile LAS u32x4*)(L + L_TS) = u32x4{tK, (unsigned)__builtin_amdgcn_s_memrealtime(), 1u, 0u};
      const unsigned dn = *(volatile LAS unsigned*)(L + L_DEC);
      SB();
      s.vround<0, R>();
      deep = __builtin_amdgcn_readfirstlane(dn);
    }
    s.vdeepen<R, R2>();
    for (; rem > 0; rem -= 8) s.vround<0, R2>();
    ST(4, 7);
    {
      const f32x2 t = fold_rows(s.acc);
      LAS float* pa = (LAS float*)(L + L_PA) + w * 128 + 8 * c + r;
      pa[0] = t[0]; pa[4] = t[1];
    }
    ST(1, 2);
    __builtin_amdgcn_s_setprio(0);
    WG_BARRIER();
    u32x4 gf[20];
    float hres[3] = {0.f, 0.f, 0.f};
    unsigned mk[8];
    const int u = lane & 7;
    row_masks(lane, mk);
    if (w != 0) gu_frags(L, 3 + w, lane, gf);
    else {
#pragma unroll
      for (int k = 0; k < 20; ++k) gf[k] = u32x4{0u, 0u, 0u, 0u};
      const rsrc_t rx = __builtin_amdgcn_make_buffer_rsrc((void*)(a.x + b * D + 20 * h), 0, 40, RS3);
#pragma unroll
      for (int e = 0; e < 3; ++e) hres[e] = bf2f(__builtin_amdgcn_raw_buffer_load_b16(rx, (3 * u + e) * 2, 0, 0));
    }
    WG_BARRIER();
    ST(1, 3); ST(3, 0);
    if (w == 0) {
      float sa[3];
      if (DELAY_A) __builtin_amdgcn_s_sleep(DELAY_A);
      gather_cols<POLL_SLEEP_FAR>(rw, WS_PA + bid * 4096 + lane * 16, tag, sa);
      ST(1, 4); ST(3, 1);
#pragma unroll
      for (int e = 0; e < 3; ++e) hres[e] = rbf(hres[e] + rbf(sa[e]));
      __builtin_amdgcn_raw_buffer_store_b128(u32x4{f2u(hres[0]), f2u(hres[1]), f2u(hres[2]), tag + 1u}, rw,
                                             lane < 8 ? WS_HB + b * HB_STRIDE + h * 128 + lane * 16 : OOB, 0, SC1);
      LAS unsigned short* hn = (LAS unsigned short*)(L + L_HN) + 20 * (lane >> 3) + 3 * u;
      LAS unsigned short* dump = (LAS unsigned short*)(L + L_PA);
      LAS unsigned short* h01 = u < 7 ? hn : dump;
      LAS unsigned short* h2 = u < 6 ? hn : dump;
      if (DELAY_B) __builtin_amdgcn_s_sleep(DELAY_B);
      ST(3, 2);
      u32x4 v[4];
      sweep<0, 0, 0>(rw, WS_HB + b * HB_STRIDE + lane * 16, tag + 1u, v);
      ST(1, 5); ST(3, 3);
#pragma unroll
      for (int i = 0; i < 4; ++i) {
        h01[160 * i] = (unsigned short)(v[i][0] >> 16);
        h01[160 * i + 1] = (unsigned short)(v[i][1] >> 16);
        h2[160 * i + 2] = (unsigned short)(v[i][2] >> 16);
      }
      ST(3, 4);
      WG_BARRIER();
      float sum = 0.f, ssq = 0.f;
#pragma unroll
      for (int i = 0; i < 4; ++i)
#pragma unroll
        for (int e = 0; e < 3; ++e) { sum += u2f(v[i][e]); ssq = fmaf(u2f(v[i][e]), u2f(v[i][e]), ssq); }
      sum = row16_sum(sum); ssq = row16_sum(ssq);
      const float sp = swap16_add(sum, ssq);
      const float sq = swap32_add(sp, sp);
      const float mean = __builtin_bit_cast(float, __builtin_amdgcn_readlane(f2u(sq), 0)) * (1.f / D);
      const float var = fmaxf(fmaf(-mean, mean, __builtin_bit_cast(float, __builtin_amdgcn_readlane(f2u(sq), 16)) * (1.f / D)), 0.f);
      *(volatile LAS u32x4*)(L + L_LN) = u32x4{f2u(rbf(mean)), f2u(inv_std(var)), 1u, 0u};
    } else {
      __builtin_amdgcn_s_sleep(CS_DELAY);
      const float cs = gu_colsum(gf);
      WG_BARRIER();
      gu_tile(L, 3 + w, lane, gf, cs);
    }
    LAS unsigned char* dst = L + L_RB + kb * 2560 + nb0 * 512 + c * 32 + (r & 1) * 16;
    WG_BARRIER();
    ST(3, 5);
    {
      const u32x4 af = *(const LAS u32x4*)(L + L_ACT + kb * 64 + r * 16);
      if (w < 2) gemv_blocks<3>(af, df, mk, dst);
      else gemv_blocks<2>(af, df, mk, dst);
    }
    ST(3, 6);
    WG_BARRIER();
    ST(1, 6);
    if (w == 0) {
      float sc[3];
      const rsrc_t ry = __builtin_amdgcn_make_buffer_rsrc((void*)(a.out + b * D + 20 * h), 0, 40, RS3);
      if (DELAY_C) __builtin_amdgcn_s_sleep(DELAY_C);
      gather_cols<0>(rw, WS_PC + bid * 4096 + lane * 16, tag + 1u, sc);
      ST(3, 7);
#pragma unroll
      for (int e = 0; e < 3; ++e) __builtin_amdgcn_raw_buffer_store_b16(f2bf(hres[e] + rbf(sc[e])), ry, lane < 8 ? (3 * u + e) * 2 : OOB, 0, 0);
      if (bid == 0 && lane == 0) __hip_atomic_store((GAS unsigned*)(a.ws + WS_SEED), tag + 1u, __ATOMIC_RELAXED, __HIP_MEMORY_SCOPE_AGENT);
    }
  } else {
    const int hw = w - 4;
    if (hw == 0) {
      const int hq = bid >> 3, ks = bid & 7;
      const rsrc_t rx = __builtin_amdgcn_make_buffer_rsrc((void*)a.x, 0, B * D * 2, RS3);
      const rsrc_t rq = __builtin_amdgcn_make_buffer_rsrc((void*)(a.wq + hq * DH), 0, (80 * (ks + 1) - 1) * (H * DH * 2) + DH * 2, RS3);
      u32x4 xs[10], xa[3], raw[3][8];
      const int xrow = (c & 7) * (D * 2);
#pragma unroll
      for (int i = 0; i < 10; ++i) xs[i] = __builtin_amdgcn_raw_buffer_load_b128(rx, (lane >> 3) * (D * 2) + (lane & 7) * 16 + i * 128, 0, 0);
#pragma unroll
      for (int k = 0; k < 3; ++k) xa[k] = __builtin_amdgcn_raw_buffer_load_b128(rx, (32 * k + 8 * r < 80) ? xrow + (80 * ks + 32 * k + 8 * r) * 2 : OOB, 0, 0);
      SB();
#pragma unroll
      for (int k = 0; k < 3; ++k)
#pragma unroll
        for (int i = 0; i < 8; ++i)
          raw[k][i] = __builtin_amdgcn_raw_buffer_load_b128(rq, 8 * r * (H * DH * 2) + c * 16, (80 * ks + 32 * k + i) * (H * DH * 2), W_AUX);
      const unsigned seed = __builtin_amdgcn_raw_buffer_load_b32(rw, WS_SEED, 0, SC1);
      SB();
      ST(2, 1);
      float sum = 0.f, ssq = 0.f;
#pragma unroll
      for (int i = 0; i < 10; ++i)
#pragma unroll
        for (int k = 0; k < 4; ++k) { sum = dot2(xs[i][k], 0x3f803f80u, sum); ssq = dot2(xs[i][k], xs[i][k], ssq); }
      sum += dpp<XOR1>(sum); ssq += dpp<XOR1>(ssq); sum += dpp<XOR2>(sum); ssq += dpp<XOR2>(ssq); sum += dpp<XOR7>(sum); ssq += dpp<XOR7>(ssq);
      const float mean = sum * (1.f / D);
      const float var = fmaxf(fmaf(-mean, mean, ssq * (1.f / D)), 0.f);
      const float mb = u2f(__builtin_amdgcn_ds_bpermute((c & 7) * 32, f2u(rbf(mean))));
      const float ri = u2f(__builtin_amdgcn_ds_bpermute((c & 7) * 32, f2u(inv_std(var))));
      u32x4 af[3];
#pragma unroll
      for (int k = 0; k < 3; ++k)
#pragma unroll
        for (int i = 0; i < 4; ++i) af[k][i] = (32 * k + 8 * r < 80) ? pk2(rbf(bf_lo(xa[k][i]) - mb) * ri, rbf(bf_hi(xa[k][i]) - mb) * ri) : 0u;
      ST(2, 2);
      f32x4 d[8];
#pragma unroll
      for (int e = 0; e < 8; ++e) d[e] = f32x4{0.f, 0.f, 0.f, 0.f};
#pragma unroll
      for (int k = 0; k < 3; ++k) {
        u32x4 fr[8];
        to_frags(raw[k], fr);
#pragma unroll
        for (int e = 0; e < 8; ++e) d[e] = mfma(af[k], fr[e], d[e]);
      }
      ST(2, 4);
      const unsigned tag = __builtin_amdgcn_readfirstlane(seed) + 1u;
      const bool up = r >= 2;
#pragma unroll
      for (int i = 0; i < 4; ++i) {
        u32x4 o;
#pragma unroll
        for (int e = 0; e < 4; ++e) o[e] = __builtin_amdgcn_perm(f2u(up ? d[4 + e][i] : d[e][i]), tag, 0x07060500u + e);
        __builtin_amdgcn_raw_buffer_store_b128(o, rw, WS_QP + ((hq * 8 + ((4 * r + i) & 7)) * 8 + ks) * 512 + (2 * c + (up ? 1 : 0)) * 16, 0, SC1);
      }
      ST(1, 4); ST(2, 5);
      __builtin_amdgcn_s_sleep(DELAY_Q);
      u32x4 v[4];
      sweep<1, 0, 4>(rw, WS_QP + (h * 8 + b) * 4096 + lane * 16, tag, v);
      ST(2, 6);
      float q[4];
#pragma unroll
      for (int e = 0; e < 4; ++e) {
        const float t = (u2f(v[0][e] & ~255u) + u2f(v[1][e] & ~255u)) + (u2f(v[2][e] & ~255u) + u2f(v[3][e] & ~255u));
        q[e] = swap32_add(t, t);
      }
      if (lane < 32) *(LAS u32x2*)(L + L_Q + lane * 8) = u32x2{pk2(q[0], q[1]), pk2(q[2], q[3])};
      if (lane == 0) *(LAS unsigned*)(L + L_TAG) = tag;
    } else if (hw == 1) {
      if (lane < 4) ((LAS unsigned*)(L + L_ACT))[28 + lane] = 0u;
      const int xcc = __builtin_amdgcn_s_getreg(20 | (3 << 11)) & 15;
      const unsigned deep0 = DEEP0 + ((xcc & 1) == 0 ? DEEP_AT : 0) + ((ch0 & 7) + nch > 8 ? DEEP_TWO : 0);
      if (lane < 28) ((LAS unsigned*)(L + L_PML))[lane] = lane == 16 ? deep0 : 0u;
    }
    ST(1, 1);
    WG_BARRIER();
    const unsigned tag = __builtin_amdgcn_readfirstlane(*(const LAS unsigned*)(L + L_TAG));
    u32x4 of[40];
    {
      const rsrc_t ro = __builtin_amdgcn_make_buffer_rsrc((void*)(a.wo + (size_t)h * DH * D), 0, DH * D * 2, RS3);
      const rsrc_t rg = __builtin_amdgcn_make_buffer_rsrc((void*)(hw < 2 ? a.wg : a.wu), 0, D * F * 2, RS3);
      const int ooff = (32 * hw + 8 * r) * (D * 2) + c * 16;
      int gi = ROT ? ((bid >> 3) * 35) >> 5 : 0;
      LAS unsigned char* dst = L + (hw < 2 ? L_G : L_U) + (hw & 1) * 35 * 1024;
#pragma unroll
      for (int n = 0; n < 5; ++n) {
        u32x4 raw[8];
#pragma unroll
        for (int i = 0; i < 8; ++i) {
          raw[i] = __builtin_amdgcn_raw_buffer_load_b128(ro, ooff + n * 256, i * (D * 2), W_AUX);
          CBAR(); __builtin_amdgcn_s_sleep(HSLEEP); CBAR();
        }
        to_frags(raw, of + 8 * n);
#pragma unroll
        for (int e = 0; e < 8; ++e) asm volatile("" : "+v"(of[8 * n + e]));
        for (int i = 0; i < 7; ++i) {
          const int id = ((hw & 1) * 35 + gi) * 64 + lane, row = (id * 9363) >> 16, pc = id - 7 * row;
          __builtin_amdgcn_raw_ptr_buffer_load_lds(rg, (LAS void*)(dst + gi * 1024), 16, pc < nch ? row * (F * 2) + (ch0 + pc) * 16 : OOB, 0, 0, W_AUX);
          gi = gi == 34 ? 0 : gi + 1;
          CBAR(); __builtin_amdgcn_s_sleep(HSLEEP); CBAR();
        }
      }
    }
    asm volatile("s_waitcnt vmcnt(0)" ::: "memory");
    ST(1, 2);
    f32x4 fw = wave_factors(L);
    if (ADVICE && hw == 1) {
      u32x4 t = {0u, 0u, 0u, 0u};
      for (int spin = 0; spin < SPIN; ++spin) {
        t = *(volatile LAS u32x4*)(L + L_TS);
        if (__builtin_amdgcn_readfirstlane(t[2])) break;
        __builtin_amdgcn_s_sleep(8);
      }
      t[3] = tag;
      __builtin_amdgcn_raw_buffer_store_b128(t, rw, lane == 0 ? WS_TS + bid * 16 : OOB, 0, SC1);
      bool all = false;
      for (int spin = 0; spin < TS_TRIES && !all; ++spin) {
        __builtin_amdgcn_s_sleep(TS_SLEEP);
        t = __builtin_amdgcn_raw_buffer_load_b128(rw, WS_TS + (b * 32 + (lane & 31)) * 16, 0, SC1);
        all = __builtin_amdgcn_ballot_w64(t[3] != tag) == 0ull;
        CBAR();
      }
      if (all) {
        const float fin = (float)(int)(t[1] - __builtin_amdgcn_readfirstlane(t[1])) + (float)(int)(t[1] - t[0]) * (BETA * (256 - CK1) / CK1);
        const float lag = u2f(__builtin_amdgcn_readlane(f2u(fin), h)) - wave_sum(fin) * (1.f / 64.f);
        int dp = (int)((lag + LAG0) * GAIN);
        dp = dp < 0 ? 0 : dp > 248 ? 248 : dp;
        if (lane == 0) *(volatile LAS unsigned*)(L + L_DEC) = (unsigned)dp;
      }
    }
    unsigned mk[8];
    row_masks(lane, mk);
    const int pj = (tid - 256) >> 3, pu = tid & 7;
    const LAS float* pp = (const LAS float*)(L + L_RB) + 20 * pj + 3 * pu;
    int poff = WS_PA + ((b * 32 + pj) * 32 + h) * 128 + pu * 16;
    unsigned m01 = pu < 7 ? ~0u : 0u, m2 = pu < 6 ? ~0u : 0u;
    const LAS f32x4* po = (const LAS f32x4*)(L + L_PA + (32 * hw + 8 * r) * 4);
    LAS unsigned char* dst = L + L_RB + hw * 2560 + c * 32 + (r & 1) * 16;
    asm volatile("" : "+v"(pp), "+v"(poff), "+v"(m01), "+v"(m2), "+v"(po), "+v"(dst), "+v"(fw));
    __builtin_amdgcn_s_setprio(1);
    WG_BARRIER();
    ST(3, 0);
    {
      const f32x4 o0 = (po[0] * fw[0] + po[32] * fw[1]) + (po[64] * fw[2] + po[96] * fw[3]);
      const f32x4 o1 = (po[1] * fw[0] + po[33] * fw[1]) + (po[65] * fw[2] + po[97] * fw[3]);
      const u32x4 af = {pk2(o0[0], o0[1]), pk2(o0[2], o0[3]), pk2(o1[0], o1[1]), pk2(o1[2], o1[3])};
      ST(3, 1);
      gemv_blocks<5>(af, of, mk, dst);
      ST(3, 2);
    }
    WG_BARRIER();
    ST(3, 3);
    publish_cols<4>(pp, rw, poff, m01, m2, tag);
    ST(1, 3); ST(3, 4);
    u32x4 gf[20];
    gu_frags(L, hw, lane, gf);
    const float cs = gu_colsum(gf);
    WG_BARRIER();
    ST(3, 5);
    gu_tile(L, hw, lane, gf, cs);
    ST(1, 5); ST(3, 6);
    WG_BARRIER();
    WG_BARRIER();
    publish_cols<2>(pp, rw, poff + (WS_PC - WS_PA), m01, m2, tag + 1u);
    ST(1, 6); ST(3, 7);
  }
}
}

extern "C" void kernel_launch(void* const* d_in, const int* in_sizes, int n_in, void* d_out, int out_size, void* d_ws,
                              size_t ws_size, hipStream_t stream) {
  Args a;
  a.x = (const unsigned short*)d_in[0];
  a.kc = (const unsigned short*)d_in[1];
  a.vc = (const unsigned short*)d_in[2];
  a.wq = (const unsigned short*)d_in[3];
  a.wo = (const unsigned short*)d_in[4];
  a.wg = (const unsigned short*)d_in[6];
  a.wu = (const unsigned short*)d_in[7];
  a.wd = (const unsigned short*)d_in[8];
  a.out = (unsigned short*)d_out;
  a.ws = (unsigned char*)d_ws;
  k_block<<<B * H, 512, 0, stream>>>(a);
}
```
